```python
import torch

def _softmax(x, axis=-1):
    return torch.softmax(x, dim=axis)
def _gelu(x):
    return torch.nn.functional.gelu(x, approximate="tanh")
def _silu(x):
    return torch.nn.functional.silu(x)
def _sigmoid(x):
    return torch.sigmoid(x)
def _layernorm(x, axis=-1, eps=1e-5):
    m = x.mean(dim=axis, keepdim=True)
    v = x.var(dim=axis, keepdim=True, unbiased=False)
    return (x - m) / torch.sqrt(v + eps)
def _scatter_add(tgt, idx, src, axis=0):
    return tgt.clone().index_add_(axis, idx, src)
def _segment_sum(src, seg, n):
    out = torch.zeros((n,) + src.shape[1:], dtype=src.dtype)
    return out.index_add_(0, seg, src)
def _pad(x, pw):
    flat = tuple(v for pair in reversed(pw) for v in pair)
    return torch.nn.functional.pad(x, flat)

kernel_name = 'concat_heads__linear_attn_block__ssm_conv_h3__prompt'

def setup_inputs():

    _w = (lambda a: (lambda t: t.to(torch.bfloat16) if t.is_floating_point() else t)(torch.as_tensor(a)))
    def _A_setup():
        B, S, D = 1, 2048, 1024
        return {
            "x": _w((lambda *s: torch.randn(*s, dtype=torch.bfloat16) * 0.05)(B, S, D)),
            "wqkv": _w((lambda *s: torch.randn(*s, dtype=torch.bfloat16) * 1.6)(D, 3 * D)),
            "wo": _w((lambda *s: torch.randn(*s, dtype=torch.bfloat16) * 0.1)(D, D)),
        }
    _A_in = _A_setup()

    def _B_setup():
        B, S, D = 1, 2048, 1024
        ks = 4
        return {
            "q": _w((lambda *s: torch.randn(*s, dtype=torch.bfloat16) * 0.05)(B, S, D)),
            "k": _w((lambda *s: torch.randn(*s, dtype=torch.bfloat16) * 0.8)(B, S, D)),
            "v": _w((lambda *s: torch.randn(*s, dtype=torch.bfloat16) * 0.4)(B, S, D)),
            "shift_k": _w((lambda *s: torch.randn(*s, dtype=torch.bfloat16) * 0.8)(D, ks) * 0.5),
            "ssm_k": _w((lambda *s: torch.randn(*s, dtype=torch.bfloat16) * 3.2)(D, S) * (1.0 / S ** 0.5)),
            "ssm_D": _w((lambda *s: torch.randn(*s, dtype=torch.bfloat16) * 0.8)(D)),
        }
    _B_in = _B_setup()

    return {
        "A_x": _A_in["x"],
        "A_wqkv": _A_in["wqkv"],
        "A_wo": _A_in["wo"],
        "B_q": _B_in["q"],
        "B_k": _B_in["k"],
        "B_v": _B_in["v"],
        "B_shift_k": _B_in["shift_k"],
        "B_ssm_k": _B_in["ssm_k"],
        "B_ssm_D": _B_in["ssm_D"],
    }

def reference(A_x, A_wqkv, A_wo, B_q, B_k, B_v, B_shift_k, B_ssm_k, B_ssm_D):
    def _A_ref(x, wqkv, wo):
        B, S, D = x.shape
        H = 32; Dh = D // H
        qkv = (lambda x, *d: x.permute(*d))((x @ wqkv).reshape(B, S, 3, H, Dh), 2, 0, 3, 1, 4)
        q, k, v = qkv[0], qkv[1], qkv[2]
        phi = lambda t: torch.where(t > 0, t + 1, torch.exp(t)) + 1e-6
        pq, pk = phi(q), phi(k)
        kv = torch.einsum("bhsd,bhse->bhde", pk, v)
        z = torch.einsum("bhsd->bhd", pk)
        num = torch.einsum("bhqd,bhde->bhqe", pq, kv)
        den = torch.einsum("bhqd,bhd->bhq", pq, z)[..., None] + 1e-6
        o = num / den
        return (lambda x, *d: x.permute(*d))(o, 0, 2, 1, 3).reshape(B, S, D) @ wo

    def _B_ref(q, k, v, shift_k, ssm_k, ssm_D):
        B, S, D = q.shape
        ks = shift_k.shape[-1]
        k_pad = _pad(k, ((0, 0), (ks - 1, 0), (0, 0)))
        k_shift = torch.zeros_like(k)
        for t in range(ks):
            k_shift = k_shift + k_pad[:, t:t + S, :] * shift_k[:, ks - 1 - t][None, None, :]
        kv = k_shift * v
        L = 2 * S
        kv_pad = _pad(kv, ((0, 0), (0, S), (0, 0)))
        kf_pad = _pad(ssm_k, ((0, 0), (0, S)))
        kv_f = (lambda x, axes=(-2,-1): torch.fft.rfft2(x.float(), dim=axes))(kv_pad[:, None, :, :], axes=(1, 2))
        kf_f = (lambda x, axes=(-2,-1): torch.fft.rfft2(x.float(), dim=axes))(kf_pad[:, None, :], axes=(1, 2))
        kf_f = (lambda x, *d: x.permute(*d))(kf_f, 1, 2, 0)
        y_f = kv_f * kf_f[None, :, :, :]
        y = (lambda x, s, axes=(-2,-1): torch.fft.irfft2(x, s=s, dim=axes).to(torch.bfloat16))(y_f, (1, L), axes=(1, 2))[:, 0, :S, :]
        y = y + kv * ssm_D[None, None, :]
        return q * y

    a = _A_ref(A_x, A_wqkv, A_wo)
    b = _B_ref(B_q, B_k, B_v, B_shift_k, B_ssm_k, B_ssm_D)
    return torch.concatenate([a, b], axis=-1)
```

```cpp
#include <hip/hip_runtime.h>
#include <hip/hip_bf16.h>
#include <stdint.h>

typedef short bf16x8 __attribute__((ext_vector_type(8)));
typedef short s16x4 __attribute__((ext_vector_type(4)));
typedef float f32x4 __attribute__((ext_vector_type(4)));
typedef unsigned short u16;

constexpr int S_LEN = 2048, DM = 1024;

__device__ __forceinline__ float bf2f(u16 v) { return __uint_as_float(((unsigned)v) << 16); }
__device__ __forceinline__ u16 f2bf(float f) {
  __hip_bfloat16 b = __float2bfloat16(f);
  return *reinterpret_cast<u16*>(&b);
}
__device__ __forceinline__ float rbf(float f) { return bf2f(f2bf(f)); }
__device__ __forceinline__ float phi(float t) {
  float r = t > 0.f ? rbf(t + 1.0f) : rbf(__expf(t));
  return rbf(r + 1e-6f);
}

constexpr int BM = 128, BK = 64, AS = 72;
template <int BN> struct GemmCfg {
  static constexpr int BS = BN + 16;
  static constexpr int LDS_A = BM * AS;
  static constexpr int LDS_B = BK * BS;
  static constexpr int NT = BN / 32;
};

template <int BN, typename ColF>
__device__ __forceinline__ void gemm_core(const u16* __restrict__ A, const u16* __restrict__ W, int ldw,
                                          int row0, ColF colbase, u16* lds, f32x4 (&acc)[4][GemmCfg<BN>::NT]) {
  using C = GemmCfg<BN>;
  constexpr int NT = C::NT;
  constexpr int BCH = BK * BN / 8 / 256;
  const int tid = threadIdx.x, lane = tid & 63, wid = tid >> 6;
  const int wr = wid >> 1, wc = wid & 1, fr = lane & 15, g = lane >> 4;
  uint4 ra[4], rb[BCH];
  auto gload = [&](int kt) {
#pragma unroll
    for (int i = 0; i < 4; ++i) {
      int c = tid + 256 * i, r = c >> 3, ch = c & 7;
      ra[i] = *reinterpret_cast<const uint4*>(A + (size_t)(row0 + r) * DM + kt * BK + ch * 8);
    }
#pragma unroll
    for (int i = 0; i < BCH; ++i) {
      int c = tid + 256 * i, r = c / (BN / 8), ch = c % (BN / 8);
      rb[i] = *reinterpret_cast<const uint4*>(W + (size_t)(kt * BK + r) * ldw + colbase(ch >> 3) + (ch & 7) * 8);
    }
  };
  auto swrite = [&](u16* la, u16* lb) {
#pragma unroll
    for (int i = 0; i < 4; ++i) {
      int c = tid + 256 * i, r = c >> 3, ch = c & 7;
      int grp = ch >> 2, cc = ch & 3;
      int half = cc >> 1, q = (cc & 1) * 2;
      u16* base = la + r * AS + grp * 32 + half * 4;
      *reinterpret_cast<uint2*>(base + 8 * q) = make_uint2(ra[i].x, ra[i].y);
      *reinterpret_cast<uint2*>(base + 8 * (q + 1)) = make_uint2(ra[i].z, ra[i].w);
    }
#pragma unroll
    for (int i = 0; i < BCH; ++i) {
      int c = tid + 256 * i, r = c / (BN / 8), ch = c % (BN / 8);
      *reinterpret_cast<uint4*>(lb + r * C::BS + ch * 8) = rb[i];
    }
  };
  constexpr int BUF = C::LDS_A + C::LDS_B;
  gload(0);
  swrite(lds, lds + C::LDS_A);
  __syncthreads();
  constexpr int NK = DM / BK;
  for (int kt = 0; kt < NK; ++kt) {
    u16* la = lds + (kt & 1) * BUF;
    u16* lb = la + C::LDS_A;
    if (kt + 1 < NK) gload(kt + 1);
#pragma unroll
    for (int ks = 0; ks < 2; ++ks) {
      bf16x8 af[4], bfr[NT];
#pragma unroll
      for (int m = 0; m < 4; ++m)
        af[m] = *reinterpret_cast<const bf16x8*>(la + (wr * 64 + m * 16 + fr) * AS + ks * 32 + g * 8);
#pragma unroll
      for (int n = 0; n < NT; ++n) {
        const int q = fr >> 2, p = fr & 3;
        const u16* pb = lb + (ks * 32 + 4 * g + q) * C::BS + wc * (BN / 2) + n * 16 + p * 4;
        s16x4 lo = __builtin_amdgcn_ds_read_tr16_b64_v4i16((__attribute__((address_space(3))) s16x4*)pb);
        s16x4 hi = __builtin_amdgcn_ds_read_tr16_b64_v4i16((__attribute__((address_space(3))) s16x4*)(pb + 16 * C::BS));
        bfr[n] = (bf16x8){lo[0], lo[1], lo[2], lo[3], hi[0], hi[1], hi[2], hi[3]};
      }
#pragma unroll
      for (int m = 0; m < 4; ++m)
#pragma unroll
        for (int n = 0; n < NT; ++n)
          acc[m][n] = __builtin_amdgcn_mfma_f32_16x16x32_bf16(af[m], bfr[n], acc[m][n], 0, 0, 0);
    }
    if (kt + 1 < NK) {
      u16* na = lds + ((kt + 1) & 1) * BUF;
      swrite(na, na + C::LDS_A);
    }
    __syncthreads();
  }
}

__global__ __launch_bounds__(256) void k_qkv(const u16* __restrict__ x, const u16* __restrict__ wqkv,
                                             u16* __restrict__ pq, float* __restrict__ part,
                                             float* __restrict__ zpart) {
  using C = GemmCfg<192>;
  __shared__ __attribute__((aligned(16))) u16 lds[2 * (C::LDS_A + C::LDS_B)];
  const int rt = blockIdx.x >> 4, p = blockIdx.x & 15;
  const int row0 = rt * BM;
  f32x4 acc[4][6];
#pragma unroll
  for (int m = 0; m < 4; ++m)
#pragma unroll
    for (int n = 0; n < 6; ++n) acc[m][n] = (f32x4){0.f, 0.f, 0.f, 0.f};
  gemm_core<192>(x, wqkv, 3 * DM, row0, [&](int seg) { return seg * DM + p * 64; }, lds, acc);

  const int lane = threadIdx.x & 63, wid = threadIdx.x >> 6;
  const int wr = wid >> 1, wc = wid & 1, fr = lane & 15, g = lane >> 4;
  constexpr int TS = 136;
  u16* pkT = lds;
  u16* vT = lds + 64 * TS;
#pragma unroll
  for (int n = 0; n < 6; ++n) {
    const int nt = wc * 6 + n, col = nt * 16 + fr;
#pragma unroll
    for (int m = 0; m < 4; ++m) {
      const int r = wr * 64 + m * 16 + g * 4;
      if (nt < 4) {
#pragma unroll
        for (int j = 0; j < 4; ++j)
          pq[(size_t)(row0 + r + j) * DM + p * 64 + col] = f2bf(phi(rbf(acc[m][n][j])));
      } else {
        u16 t[4];
#pragma unroll
        for (int j = 0; j < 4; ++j) {
          float v = rbf(acc[m][n][j]);
          t[j] = f2bf(nt < 8 ? phi(v) : v);
        }
        u16* dst = (nt < 8 ? pkT + (col - 64) * TS : vT + (col - 128) * TS) + r;
        *reinterpret_cast<uint2*>(dst) = make_uint2(t[0] | ((unsigned)t[1] << 16), t[2] | ((unsigned)t[3] << 16));
      }
    }
  }
  __syncthreads();
  {
    const int hh = wid >> 1;
    f32x4 c0 = {0, 0, 0, 0}, c1 = {0, 0, 0, 0}, cz = {0, 0, 0, 0};
    const short one = (short)0x3F80;
    const bf16x8 ones = {one, one, one, one, one, one, one, one};
#pragma unroll
    for (int ks = 0; ks < 4; ++ks) {
      bf16x8 a = *reinterpret_cast<const bf16x8*>(pkT + (wid * 16 + fr) * TS + ks * 32 + g * 8);
      bf16x8 b0 = *reinterpret_cast<const bf16x8*>(vT + (hh * 32 + fr) * TS + ks * 32 + g * 8);
      bf16x8 b1 = *reinterpret_cast<const bf16x8*>(vT + (hh * 32 + 16 + fr) * TS + ks * 32 + g * 8);
      c0 = __builtin_amdgcn_mfma_f32_16x16x32_bf16(a, b0, c0, 0, 0, 0);
      c1 = __builtin_amdgcn_mfma_f32_16x16x32_bf16(a, b1, c1, 0, 0, 0);
      cz = __builtin_amdgcn_mfma_f32_16x16x32_bf16(a, ones, cz, 0, 0, 0);
    }
    const int head = p * 2 + hh;
    float* pp = part + ((size_t)rt * 32 + head) * 1024;
#pragma unroll
    for (int j = 0; j < 4; ++j) {
      const int d = (wid & 1) * 16 + g * 4 + j;
      pp[d * 32 + fr] = c0[j];
      pp[d * 32 + 16 + fr] = c1[j];
      if (fr == 0) zpart[((size_t)rt * 32 + head) * 32 + d] = cz[j];
    }
  }
}

__global__ __launch_bounds__(256) void k_attn_o(const u16* __restrict__ pq, const float* __restrict__ part,
                                                const float* __restrict__ zpart, u16* __restrict__ o) {
  __shared__ float kv[2][32][33];
  __shared__ float z[2][32];
  const int rt = blockIdx.x >> 4, p = blockIdx.x & 15, tid = threadIdx.x;
  for (int i = tid; i < 2048; i += 256) {
    int hh = i >> 10, e = i & 1023;
    float s = 0.f;
#pragma unroll
    for (int t = 0; t < 16; ++t) s += part[((size_t)t * 32 + p * 2 + hh) * 1024 + e];
    kv[hh][e >> 5][e & 31] = rbf(s);
  }
  if (tid < 64) {
    float s = 0.f;
#pragma unroll
    for (int t = 0; t < 16; ++t) s += zpart[((size_t)t * 32 + p * 2) * 32 + tid];
    z[tid >> 5][tid & 31] = rbf(s);
  }
  __syncthreads();
  const int row = rt * 128 + (tid >> 1), hh = tid & 1;
  const u16* q = pq + (size_t)row * DM + p * 64 + hh * 32;
  float qf[32];
#pragma unroll
  for (int i = 0; i < 4; ++i) {
    uint4 v = *reinterpret_cast<const uint4*>(q + i * 8);
    unsigned w[4] = {v.x, v.y, v.z, v.w};
#pragma unroll
    for (int j = 0; j < 4; ++j) {
      qf[i * 8 + 2 * j] = __uint_as_float(w[j] << 16);
      qf[i * 8 + 2 * j + 1] = __uint_as_float(w[j] & 0xffff0000u);
    }
  }
  float den = 0.f;
#pragma unroll
  for (int d = 0; d < 32; ++d) den += qf[d] * z[hh][d];
  den = rbf(rbf(den) + 1e-6f);
  unsigned ow[16];
#pragma unroll
  for (int e = 0; e < 32; e += 2) {
    float n0 = 0.f, n1 = 0.f;
#pragma unroll
    for (int d = 0; d < 32; ++d) { n0 += qf[d] * kv[hh][d][e]; n1 += qf[d] * kv[hh][d][e + 1]; }
    ow[e >> 1] = f2bf(rbf(n0) / den) | ((unsigned)f2bf(rbf(n1) / den) << 16);
  }
  u16* op = o + (size_t)row * DM + p * 64 + hh * 32;
#pragma unroll
  for (int i = 0; i < 4; ++i)
    *reinterpret_cast<uint4*>(op + i * 8) = make_uint4(ow[4 * i], ow[4 * i + 1], ow[4 * i + 2], ow[4 * i + 3]);
}

__global__ __launch_bounds__(256) void k_out(const u16* __restrict__ o, const u16* __restrict__ wo,
                                             u16* __restrict__ out) {
  using C = GemmCfg<64>;
  __shared__ __attribute__((aligned(16))) u16 lds[2 * (C::LDS_A + C::LDS_B)];
  const int rt = blockIdx.x >> 4, ct = blockIdx.x & 15;
  f32x4 acc[4][2];
#pragma unroll
  for (int m = 0; m < 4; ++m)
#pragma unroll
    for (int n = 0; n < 2; ++n) acc[m][n] = (f32x4){0.f, 0.f, 0.f, 0.f};
  gemm_core<64>(o, wo, DM, rt * BM, [&](int) { return ct * 64; }, lds, acc);
  const int lane = threadIdx.x & 63, wid = threadIdx.x >> 6;
  const int wr = wid >> 1, wc = wid & 1, fr = lane & 15, g = lane >> 4;
#pragma unroll
  for (int m = 0; m < 4; ++m)
#pragma unroll
    for (int n = 0; n < 2; ++n)
#pragma unroll
      for (int j = 0; j < 4; ++j)
        out[(size_t)(rt * BM + wr * 64 + m * 16 + g * 4 + j) * (2 * DM) + ct * 64 + wc * 32 + n * 16 + fr] =
            f2bf(acc[m][n][j]);
}

constexpr int HR = 2136;
__global__ __launch_bounds__(512) void k_conv(const u16* __restrict__ q, const u16* __restrict__ k,
                                              const u16* __restrict__ v, const u16* __restrict__ shift_k,
                                              const u16* __restrict__ ssm_k, const u16* __restrict__ ssm_D,
                                              u16* __restrict__ out) {
  __shared__ __attribute__((aligned(16))) u16 kvT[8][S_LEN];
  __shared__ __attribute__((aligned(16))) u16 hr[8][2][HR];
  __shared__ __attribute__((aligned(16))) u16 zrow[64];
  const int c0 = blockIdx.x * 8, tid = threadIdx.x, lane = tid & 63, w = tid >> 6;
  if (tid < 64) zrow[tid] = 0;
  for (int i = lane; i < HR; i += 64) {
    int i0 = 2064 - i, i1 = 2064 - i - 1;
    hr[w][0][i] = (i0 >= 0 && i0 < S_LEN) ? ssm_k[(size_t)(c0 + w) * S_LEN + i0] : (u16)0;
    hr[w][1][i] = (i1 >= 0 && i1 < S_LEN) ? ssm_k[(size_t)(c0 + w) * S_LEN + i1] : (u16)0;
  }
  {
    const int t0 = tid * 4;
    float kf[7][8];
#pragma unroll
    for (int i = 0; i < 7; ++i) {
      int t = t0 - 3 + i;
      uint4 u = make_uint4(0, 0, 0, 0);
      if (t >= 0) u = *reinterpret_cast<const uint4*>(k + (size_t)t * DM + c0);
      unsigned ww[4] = {u.x, u.y, u.z, u.w};
#pragma unroll
      for (int j = 0; j < 4; ++j) {
        kf[i][2 * j] = __uint_as_float(ww[j] << 16);
        kf[i][2 * j + 1] = __uint_as_float(ww[j] & 0xffff0000u);
      }
    }
    u16 res[8][4];
#pragma unroll
    for (int r = 0; r < 4; ++r) {
      uint4 u = *reinterpret_cast<const uint4*>(v + (size_t)(t0 + r) * DM + c0);
      unsigned ww[4] = {u.x, u.y, u.z, u.w};
#pragma unroll
      for (int c = 0; c < 8; ++c) {
        float vf = (c & 1) ? __uint_as_float(ww[c >> 1] & 0xffff0000u) : __uint_as_float(ww[c >> 1] << 16);
        float ks = 0.f;
#pragma unroll
        for (int t = 0; t < 4; ++t)
          ks = rbf(ks + rbf(kf[r + t][c] * bf2f(shift_k[(c0 + c) * 4 + 3 - t])));
        res[c][r] = f2bf(ks * vf);
      }
    }
#pragma unroll
    for (int c = 0; c < 8; ++c)
      *reinterpret_cast<uint2*>(&kvT[c][t0]) =
          make_uint2(res[c][0] | ((unsigned)res[c][1] << 16), res[c][2] | ((unsigned)res[c][3] << 16));
  }
  __syncthreads();
  const int n = lane & 15, g = lane >> 4;
  f32x4 acc[2][4];
#pragma unroll
  for (int a = 0; a < 2; ++a)
#pragma unroll
    for (int b = 0; b < 4; ++b) acc[a][b] = (f32x4){0.f, 0.f, 0.f, 0.f};
  const u16* hbase = &hr[w][n & 1][2064 - n - (n & 1) + 8 * g];
  for (int jj = 0; jj < 32; ++jj) {
    bf16x8 bfr[6];
#pragma unroll
    for (int f = 0; f < 6; ++f) {
      const unsigned* hp = reinterpret_cast<const unsigned*>(hbase - (64 * jj + 16 * f - 32));
      unsigned a0 = hp[0], a1 = hp[1], a2 = hp[2], a3 = hp[3];
      uint4 u = make_uint4(a0, a1, a2, a3);
      bfr[f] = *reinterpret_cast<bf16x8*>(&u);
    }
#pragma unroll
    for (int ab = 0; ab < 2; ++ab) {
      if (ab == 0 && jj >= 16) continue;
      const int a = ab * 16 + n - jj;
#pragma unroll
      for (int kb = 0; kb < 2; ++kb) {
        const u16* ap = (a >= 0) ? &kvT[w][a * 64 + kb * 32 + g * 8] : &zrow[g * 8];
        bf16x8 af = *reinterpret_cast<const bf16x8*>(ap);
#pragma unroll
        for (int nb = 0; nb < 4; ++nb)
          acc[ab][nb] = __builtin_amdgcn_mfma_f32_16x16x32_bf16(af, bfr[nb - 2 * kb + 2], acc[ab][nb], 0, 0, 0);
      }
    }
  }
  __syncthreads();
  u16* yT = &hr[w][0][0];
#pragma unroll
  for (int ab = 0; ab < 2; ++ab)
#pragma unroll
    for (int nb = 0; nb < 4; ++nb)
#pragma unroll
      for (int j = 0; j < 4; ++j) yT[(ab * 16 + g * 4 + j) * 64 + nb * 16 + n] = f2bf(acc[ab][nb][j]);
  __syncthreads();
  {
    const int t0 = tid * 4;
    u16 yy[8][4], kk[8][4];
#pragma unroll
    for (int c = 0; c < 8; ++c) {
      uint2 a = *reinterpret_cast<const uint2*>(&hr[c][0][t0]);
      uint2 b = *reinterpret_cast<const uint2*>(&kvT[c][t0]);
      yy[c][0] = a.x & 0xffff; yy[c][1] = a.x >> 16; yy[c][2] = a.y & 0xffff; yy[c][3] = a.y >> 16;
      kk[c][0] = b.x & 0xffff; kk[c][1] = b.x >> 16; kk[c][2] = b.y & 0xffff; kk[c][3] = b.y >> 16;
    }
#pragma unroll
    for (int r = 0; r < 4; ++r) {
      uint4 u = *reinterpret_cast<const uint4*>(q + (size_t)(t0 + r) * DM + c0);
      unsigned ww[4] = {u.x, u.y, u.z, u.w}, ow[4];
      u16 res[8];
#pragma unroll
      for (int c = 0; c < 8; ++c) {
        float qf = (c & 1) ? __uint_as_float(ww[c >> 1] & 0xffff0000u) : __uint_as_float(ww[c >> 1] << 16);
        float y2 = rbf(bf2f(yy[c][r]) + rbf(bf2f(kk[c][r]) * bf2f(ssm_D[c0 + c])));
        res[c] = f2bf(qf * y2);
      }
#pragma unroll
      for (int j = 0; j < 4; ++j) ow[j] = res[2 * j] | ((unsigned)res[2 * j + 1] << 16);
      *reinterpret_cast<uint4*>(out + (size_t)(t0 + r) * (2 * DM) + DM + c0) = make_uint4(ow[0], ow[1], ow[2], ow[3]);
    }
  }
}

extern "C" void kernel_launch(void* const* d_in, const int* in_sizes, int n_in, void* d_out, int out_size,
                              void* d_ws, size_t ws_size, hipStream_t stream) {
  const u16* x = (const u16*)d_in[0];
  const u16* wqkv = (const u16*)d_in[1];
  const u16* wo = (const u16*)d_in[2];
  u16* out = (u16*)d_out;
  char* ws = (char*)d_ws;
  u16* pq = (u16*)ws;
  u16* o = (u16*)(ws + (4u << 20));
  float* part = (float*)(ws + (8u << 20));
  float* zpart = (float*)(ws + (10u << 20));
  k_conv<<<128, 512, 0, stream>>>((const u16*)d_in[3], (const u16*)d_in[4], (const u16*)d_in[5],
                                  (const u16*)d_in[6], (const u16*)d_in[7], (const u16*)d_in[8], out);
  k_qkv<<<256, 256, 0, stream>>>(x, wqkv, pq, part, zpart);
  k_attn_o<<<256, 256, 0, stream>>>(pq, part, zpart, o);
  k_out<<<256, 256, 0, stream>>>(o, wo, out);
}
```

```cpp
#include <hip/hip_runtime.h>
#include <hip/hip_bf16.h>
#include <stdint.h>

typedef short bf16x8 __attribute__((ext_vector_type(8)));
typedef short s16x4 __attribute__((ext_vector_type(4)));
typedef float f32x4 __attribute__((ext_vector_type(4)));
typedef unsigned short u16;

constexpr int S_LEN = 2048, DM = 1024;

__device__ __forceinline__ float bf2f(u16 v) { return __uint_as_float(((unsigned)v) << 16); }
__device__ __forceinline__ u16 f2bf(float f) {
  __hip_bfloat16 b = __float2bfloat16(f);
  return *reinterpret_cast<u16*>(&b);
}
__device__ __forceinline__ float rbf(float f) { return bf2f(f2bf(f)); }
__device__ __forceinline__ float phi(float t) {
  float r = t > 0.f ? rbf(t + 1.0f) : rbf(__expf(t));
  return rbf(r + 1e-6f);
}

constexpr int BM = 128, BK = 64;
template <int BN, int WN, int ST> struct GemmCfg {
  static constexpr int NW = 2 * WN;
  static constexpr int NT = BN / WN / 16;
  static constexpr int A_BYTES = BM * BK * 2;
  static constexpr int B_BYTES = BK * BN * 2;
  static constexpr int STAGE = A_BYTES + B_BYTES;
  static constexpr int AI = A_BYTES / 1024 / NW;
  static constexpr int BI = B_BYTES / 1024 / NW;
  static constexpr int LDS_BYTES = ST * STAGE;
};
#define WAITV(n) asm volatile("s_waitcnt vmcnt(%0)" ::"n"(n) : "memory")

template <int BN, int WN, int ST, typename ColF>
__device__ __forceinline__ void gemm_core(const u16* __restrict__ A, const u16* __restrict__ W, int ldw,
                                          int row0, ColF colbase, char* lds,
                                          f32x4 (&acc)[4][GemmCfg<BN, WN, ST>::NT]) {
  using C = GemmCfg<BN, WN, ST>;
  constexpr int NT = C::NT, NK = DM / BK;
  const int tid = threadIdx.x, lane = tid & 63, wid = tid >> 6;
  const int wr = wid / WN, wc = wid % WN, fr = lane & 15, g = lane >> 4;
  const u16* asrc[C::AI];
  const u16* bsrc[C::BI];
#pragma unroll
  for (int i = 0; i < C::AI; ++i) {
    int pos = (wid * C::AI + i) * 64 + lane;
    int r = pos >> 3, c = pos & 7;
    asrc[i] = A + (size_t)(row0 + r) * DM + ((c ^ (r & 7)) * 8);
  }
#pragma unroll
  for (int i = 0; i < C::BI; ++i) {
    int pos = (wid * C::BI + i) * 64 + lane;
    int seg = pos >> 9, r = (pos >> 3) & 63, c = pos & 7;
    int f = ((r >> 1) & 1) | (((r >> 3) & 1) << 1);
    int cs = (((c >> 1) ^ f) << 1) | (c & 1);
    bsrc[i] = W + (size_t)r * ldw + colbase(seg) + cs * 8;
  }
  auto issue = [&](int kt) {
    char* sb = lds + (kt % ST) * C::STAGE;
#pragma unroll
    for (int i = 0; i < C::AI; ++i)
      __builtin_amdgcn_global_load_lds((const unsigned*)(asrc[i] + kt * BK),
                                       (__attribute__((address_space(3))) unsigned*)(sb + (wid * C::AI + i) * 1024), 16, 0, 0);
#pragma unroll
    for (int i = 0; i < C::BI; ++i)
      __builtin_amdgcn_global_load_lds((const unsigned*)(bsrc[i] + (size_t)kt * BK * ldw),
                                       (__attribute__((address_space(3))) unsigned*)(sb + C::A_BYTES + (wid * C::BI + i) * 1024), 16, 0, 0);
  };
#pragma unroll
  for (int s = 0; s < ST - 1; ++s) issue(s);
  int aoff[4][2];
#pragma unroll
  for (int m = 0; m < 4; ++m)
#pragma unroll
    for (int ks = 0; ks < 2; ++ks) {
      int r = wr * 64 + m * 16 + fr;
      aoff[m][ks] = r * 128 + (((ks * 4 + g) ^ (r & 7)) * 16);
    }
  int boff[NT];
#pragma unroll
  for (int n = 0; n < NT; ++n) {
    int col = wc * (BN / WN) + n * 16, seg = col >> 6, cp = (col & 63) >> 4;
    int r = 8 * g + (fr >> 2), p = fr & 3;
    int f = ((r >> 1) & 1) | (((r >> 3) & 1) << 1);
    boff[n] = C::A_BYTES + seg * 8192 + r * 128 + ((cp ^ f) * 32) + p * 8;
  }
  constexpr int PER = C::AI + C::BI;
#pragma unroll 1
  for (int kt = 0; kt < NK; ++kt) {
    if (kt + ST - 2 < NK) WAITV(PER * (ST - 2)); else WAITV(0);
    __builtin_amdgcn_s_barrier();
    if (kt + ST - 1 < NK) issue(kt + ST - 1);
    const char* sb = lds + (kt % ST) * C::STAGE;
#pragma unroll
    for (int ks = 0; ks < 2; ++ks) {
      bf16x8 af[4], bfr[NT];
#pragma unroll
      for (int m = 0; m < 4; ++m) af[m] = *reinterpret_cast<const bf16x8*>(sb + aoff[m][ks]);
#pragma unroll
      for (int n = 0; n < NT; ++n) {
        const char* pb = sb + boff[n] + ks * 32 * 128;
        s16x4 lo = __builtin_amdgcn_ds_read_tr16_b64_v4i16((__attribute__((address_space(3))) s16x4*)pb);
        s16x4 hi = __builtin_amdgcn_ds_read_tr16_b64_v4i16((__attribute__((address_space(3))) s16x4*)(pb + 4 * 128));
        bfr[n] = (bf16x8){lo[0], lo[1], lo[2], lo[3], hi[0], hi[1], hi[2], hi[3]};
      }
#pragma unroll
      for (int m = 0; m < 4; ++m)
#pragma unroll
        for (int n = 0; n < NT; ++n)
          acc[m][n] = __builtin_amdgcn_mfma_f32_16x16x32_bf16(af[m], bfr[n], acc[m][n], 0, 0, 0);
    }
  }
  __syncthreads();
}

__global__ __launch_bounds__(512) void k_qkv(const u16* __restrict__ x, const u16* __restrict__ wqkv,
                                             u16* __restrict__ pq, float* __restrict__ part,
                                             float* __restrict__ zpart) {
  using C = GemmCfg<192, 4, 3>;
  __shared__ __attribute__((aligned(16))) char lds[C::LDS_BYTES];
  const int rt = blockIdx.x >> 4, p = blockIdx.x & 15;
  const int row0 = rt * BM;
  f32x4 acc[4][3];
#pragma unroll
  for (int m = 0; m < 4; ++m)
#pragma unroll
    for (int n = 0; n < 3; ++n) acc[m][n] = (f32x4){0.f, 0.f, 0.f, 0.f};
  gemm_core<192, 4, 3>(x, wqkv, 3 * DM, row0, [&](int seg) { return seg * DM + p * 64; }, lds, acc);

  const int lane = threadIdx.x & 63, wid = threadIdx.x >> 6;
  const int wr = wid >> 2, wc = wid & 3, fr = lane & 15, g = lane >> 4;
  constexpr int TS = 136;
  u16* pkT = (u16*)lds;
  u16* vT = pkT + 64 * TS;
  u16* pqs = vT + 64 * TS;
#pragma unroll
  for (int n = 0; n < 3; ++n) {
    const int nt = wc * 3 + n, col = nt * 16 + fr;
#pragma unroll
    for (int m = 0; m < 4; ++m) {
      const int r = wr * 64 + m * 16 + g * 4;
      u16 t[4];
#pragma unroll
      for (int j = 0; j < 4; ++j) {
        float v = rbf(acc[m][n][j]);
        t[j] = f2bf(nt < 8 ? phi(v) : v);
      }
      if (nt < 4) {
#pragma unroll
        for (int j = 0; j < 4; ++j) pqs[(r + j) * 72 + col] = t[j];
      } else {
        u16* dst = (nt < 8 ? pkT + (col - 64) * TS : vT + (col - 128) * TS) + r;
        *reinterpret_cast<uint2*>(dst) = make_uint2(t[0] | ((unsigned)t[1] << 16), t[2] | ((unsigned)t[3] << 16));
      }
    }
  }
  __syncthreads();
#pragma unroll
  for (int i = 0; i < 2; ++i) {
    int c = threadIdx.x + 512 * i, r = c >> 3, ch = c & 7;
    *reinterpret_cast<uint4*>(pq + (size_t)(row0 + r) * DM + p * 64 + ch * 8) =
        *reinterpret_cast<const uint4*>(pqs + r * 72 + ch * 8);
  }
  if (wid < 4) {
    const int hh = wid >> 1;
    f32x4 c0 = {0, 0, 0, 0}, c1 = {0, 0, 0, 0}, cz = {0, 0, 0, 0};
    const short one = (short)0x3F80;
    const bf16x8 ones = {one, one, one, one, one, one, one, one};
#pragma unroll
    for (int ks = 0; ks < 4; ++ks) {
      bf16x8 a = *reinterpret_cast<const bf16x8*>(pkT + (wid * 16 + fr) * TS + ks * 32 + g * 8);
      bf16x8 b0 = *reinterpret_cast<const bf16x8*>(vT + (hh * 32 + fr) * TS + ks * 32 + g * 8);
      bf16x8 b1 = *reinterpret_cast<const bf16x8*>(vT + (hh * 32 + 16 + fr) * TS + ks * 32 + g * 8);
      c0 = __builtin_amdgcn_mfma_f32_16x16x32_bf16(a, b0, c0, 0, 0, 0);
      c1 = __builtin_amdgcn_mfma_f32_16x16x32_bf16(a, b1, c1, 0, 0, 0);
      cz = __builtin_amdgcn_mfma_f32_16x16x32_bf16(a, ones, cz, 0, 0, 0);
    }
    const int head = p * 2 + hh;
    float* pp = part + ((size_t)rt * 32 + head) * 1024;
#pragma unroll
    for (int j = 0; j < 4; ++j) {
      const int d = (wid & 1) * 16 + g * 4 + j;
      pp[d * 32 + fr] = c0[j];
      pp[d * 32 + 16 + fr] = c1[j];
      if (fr == 0) zpart[((size_t)rt * 32 + head) * 32 + d] = cz[j];
    }
  }
}

__global__ __launch_bounds__(256) void k_attn_o(const u16* __restrict__ pq, const float* __restrict__ part,
                                                const float* __restrict__ zpart, u16* __restrict__ o) {
  __shared__ __attribute__((aligned(16))) u16 kvT[2][32][40];
  __shared__ __attribute__((aligned(16))) u16 zs[2][32];
  const int rt = blockIdx.x >> 4, p = blockIdx.x & 15, tid = threadIdx.x;
#pragma unroll
  for (int i = 0; i < 2; ++i) {
    int c = tid + 256 * i;
    int hh = c >> 8, d = (c >> 3) & 31, e4 = (c & 7) * 4;
    float4 s = make_float4(0, 0, 0, 0);
#pragma unroll
    for (int t = 0; t < 16; ++t) {
      float4 v = *reinterpret_cast<const float4*>(part + ((size_t)t * 32 + p * 2 + hh) * 1024 + d * 32 + e4);
      s.x += v.x; s.y += v.y; s.z += v.z; s.w += v.w;
    }
    kvT[hh][e4][d] = f2bf(s.x); kvT[hh][e4 + 1][d] = f2bf(s.y);
    kvT[hh][e4 + 2][d] = f2bf(s.z); kvT[hh][e4 + 3][d] = f2bf(s.w);
  }
  if (tid < 64) {
    float s = 0.f;
#pragma unroll
    for (int t = 0; t < 16; ++t) s += zpart[((size_t)t * 32 + p * 2) * 32 + tid];
    zs[tid >> 5][tid & 31] = f2bf(s);
  }
  __syncthreads();
  const int lane = tid & 63, wid = tid >> 6, fr = lane & 15, g = lane >> 4;
#pragma unroll
  for (int rb = 0; rb < 2; ++rb) {
    const int row = rt * 128 + wid * 32 + rb * 16 + fr;
#pragma unroll
    for (int hh = 0; hh < 2; ++hh) {
      bf16x8 b = *reinterpret_cast<const bf16x8*>(pq + (size_t)row * DM + p * 64 + hh * 32 + g * 8);
      bf16x8 a0 = *reinterpret_cast<const bf16x8*>(&kvT[hh][fr][g * 8]);
      bf16x8 a1 = *reinterpret_cast<const bf16x8*>(&kvT[hh][16 + fr][g * 8]);
      bf16x8 az = *reinterpret_cast<const bf16x8*>(&zs[hh][g * 8]);
      f32x4 z4 = {0, 0, 0, 0};
      f32x4 n0 = __builtin_amdgcn_mfma_f32_16x16x32_bf16(a0, b, z4, 0, 0, 0);
      f32x4 n1 = __builtin_amdgcn_mfma_f32_16x16x32_bf16(a1, b, z4, 0, 0, 0);
      f32x4 dn = __builtin_amdgcn_mfma_f32_16x16x32_bf16(az, b, z4, 0, 0, 0);
      const float den = rbf(rbf(dn[0]) + 1e-6f);
      u16 r0[4], r1[4];
#pragma unroll
      for (int j = 0; j < 4; ++j) { r0[j] = f2bf(rbf(n0[j]) / den); r1[j] = f2bf(rbf(n1[j]) / den); }
      u16* op = o + (size_t)row * DM + p * 64 + hh * 32 + g * 4;
      *reinterpret_cast<uint2*>(op) = make_uint2(r0[0] | ((unsigned)r0[1] << 16), r0[2] | ((unsigned)r0[3] << 16));
      *reinterpret_cast<uint2*>(op + 16) = make_uint2(r1[0] | ((unsigned)r1[1] << 16), r1[2] | ((unsigned)r1[3] << 16));
    }
  }
}

__global__ __launch_bounds__(256) void k_out(const u16* __restrict__ o, const u16* __restrict__ wo,
                                             u16* __restrict__ out) {
  using C = GemmCfg<64, 2, 4>;
  __shared__ __attribute__((aligned(16))) char lds[C::LDS_BYTES];
  const int rt = blockIdx.x >> 4, ct = blockIdx.x & 15;
  f32x4 acc[4][2];
#pragma unroll
  for (int m = 0; m < 4; ++m)
#pragma unroll
    for (int n = 0; n < 2; ++n) acc[m][n] = (f32x4){0.f, 0.f, 0.f, 0.f};
  gemm_core<64, 2, 4>(o, wo, DM, rt * BM, [&](int) { return ct * 64; }, lds, acc);
  const int lane = threadIdx.x & 63, wid = threadIdx.x >> 6;
  const int wr = wid >> 1, wc = wid & 1, fr = lane & 15, g = lane >> 4;
  u16* os = (u16*)lds;
#pragma unroll
  for (int m = 0; m < 4; ++m)
#pragma unroll
    for (int n = 0; n < 2; ++n)
#pragma unroll
      for (int j = 0; j < 4; ++j)
        os[(wr * 64 + m * 16 + g * 4 + j) * 72 + wc * 32 + n * 16 + fr] = f2bf(acc[m][n][j]);
  __syncthreads();
#pragma unroll
  for (int i = 0; i < 4; ++i) {
    int c = threadIdx.x + 256 * i, r = c >> 3, ch = c & 7;
    *reinterpret_cast<uint4*>(out + (size_t)(rt * BM + r) * (2 * DM) + ct * 64 + ch * 8) =
        *reinterpret_cast<const uint4*>(os + r * 72 + ch * 8);
  }
}

constexpr int HR = 2136;
__global__ __launch_bounds__(512) void k_conv(const u16* __restrict__ q, const u16* __restrict__ k,
                                              const u16* __restrict__ v, const u16* __restrict__ shift_k,
                                              const u16* __restrict__ ssm_k, const u16* __restrict__ ssm_D,
                                              u16* __restrict__ out) {
  __shared__ __attribute__((aligned(16))) u16 kvT[8][S_LEN];
  __shared__ __attribute__((aligned(16))) u16 hr[8][2][HR];
  __shared__ __attribute__((aligned(16))) u16 zrow[64];
  const int c0 = blockIdx.x * 8, tid = threadIdx.x, lane = tid & 63, w = tid >> 6;
  if (tid < 64) zrow[tid] = 0;
  {
    const u16* hsrc = ssm_k + (size_t)(c0 + w) * S_LEN;
    uint4 u[4]; u16 nx[4];
#pragma unroll
    for (int i = 0; i < 4; ++i) {
      int idx0 = (lane + 64 * i) * 8;
      u[i] = *reinterpret_cast<const uint4*>(hsrc + idx0);
      nx[i] = (idx0 + 8 < S_LEN) ? hsrc[idx0 + 8] : (u16)0;
    }
    if (lane < 16) { hr[w][0][lane] = 0; hr[w][1][lane] = 0; }
    hr[w][0][2064 + lane] = 0; hr[w][1][2064 + lane] = 0;
    if (lane < HR - 2128) { hr[w][0][2128 + lane] = 0; hr[w][1][2128 + lane] = 0; }
#pragma unroll
    for (int i = 0; i < 4; ++i) {
      int idx0 = (lane + 64 * i) * 8;
      auto rev = [](unsigned a) { return (a >> 16) | (a << 16); };
      uint4 r0 = make_uint4(rev(u[i].w), rev(u[i].z), rev(u[i].y), rev(u[i].x));
      *reinterpret_cast<uint4*>(&hr[w][0][2056 - idx0]) = r0;
      uint4 r1 = make_uint4((r0.x << 16) | nx[i], (r0.y << 16) | (r0.x >> 16), (r0.z << 16) | (r0.y >> 16),
                            (r0.w << 16) | (r0.z >> 16));
      *reinterpret_cast<uint4*>(&hr[w][1][2056 - idx0]) = r1;
    }
    if (lane == 0) hr[w][1][2064] = hsrc[0];
  }
  {
    const int t0 = tid * 4;
    float kf[7][8];
#pragma unroll
    for (int i = 0; i < 7; ++i) {
      int t = t0 - 3 + i;
      uint4 u = make_uint4(0, 0, 0, 0);
      if (t >= 0) u = *reinterpret_cast<const uint4*>(k + (size_t)t * DM + c0);
      unsigned ww[4] = {u.x, u.y, u.z, u.w};
#pragma unroll
      for (int j = 0; j < 4; ++j) {
        kf[i][2 * j] = __uint_as_float(ww[j] << 16);
        kf[i][2 * j + 1] = __uint_as_float(ww[j] & 0xffff0000u);
      }
    }
    u16 res[8][4];
#pragma unroll
    for (int r = 0; r < 4; ++r) {
      uint4 u = *reinterpret_cast<const uint4*>(v + (size_t)(t0 + r) * DM + c0);
      unsigned ww[4] = {u.x, u.y, u.z, u.w};
#pragma unroll
      for (int c = 0; c < 8; ++c) {
        float vf = (c & 1) ? __uint_as_float(ww[c >> 1] & 0xffff0000u) : __uint_as_float(ww[c >> 1] << 16);
        float ks = 0.f;
#pragma unroll
        for (int t = 0; t < 4; ++t)
          ks = rbf(ks + rbf(kf[r + t][c] * bf2f(shift_k[(c0 + c) * 4 + 3 - t])));
        res[c][r] = f2bf(ks * vf);
      }
    }
#pragma unroll
    for (int c = 0; c < 8; ++c)
      *reinterpret_cast<uint2*>(&kvT[c][t0]) =
          make_uint2(res[c][0] | ((unsigned)res[c][1] << 16), res[c][2] | ((unsigned)res[c][3] << 16));
  }
  __syncthreads();
  const int n = lane & 15, g = lane >> 4;
  f32x4 acc[2][4];
#pragma unroll
  for (int a = 0; a < 2; ++a)
#pragma unroll
    for (int b = 0; b < 4; ++b) acc[a][b] = (f32x4){0.f, 0.f, 0.f, 0.f};
  const u16* hbase = &hr[w][(n & 1) ^ 1][2064 - n - (n & 1) + 8 * g];
  for (int jj = 0; jj < 32; ++jj) {
    bf16x8 bfr[6];
#pragma unroll
    for (int f = 0; f < 6; ++f) {
      const unsigned* hp = reinterpret_cast<const unsigned*>(hbase - (64 * jj + 16 * f - 32));
      unsigned a0 = hp[0], a1 = hp[1], a2 = hp[2], a3 = hp[3];
      uint4 u = make_uint4(a0, a1, a2, a3);
      bfr[f] = *reinterpret_cast<bf16x8*>(&u);
    }
#pragma unroll
    for (int ab = 0; ab < 2; ++ab) {
      if (ab == 0 && jj >= 16) continue;
      const int a = ab * 16 + n - jj;
#pragma unroll
      for (int kb = 0; kb < 2; ++kb) {
        const u16* ap = (a >= 0) ? &kvT[w][a * 64 + kb * 32 + g * 8] : &zrow[g * 8];
        bf16x8 af = *reinterpret_cast<const bf16x8*>(ap);
#pragma unroll
        for (int nb = 0; nb < 4; ++nb)
          acc[ab][nb] = __builtin_amdgcn_mfma_f32_16x16x32_bf16(af, bfr[nb - 2 * kb + 2], acc[ab][nb], 0, 0, 0);
      }
    }
  }
  __syncthreads();
  u16* yT = &hr[w][0][0];
#pragma unroll
  for (int ab = 0; ab < 2; ++ab)
#pragma unroll
    for (int nb = 0; nb < 4; ++nb)
#pragma unroll
      for (int j = 0; j < 4; ++j) yT[(ab * 16 + g * 4 + j) * 64 + nb * 16 + n] = f2bf(acc[ab][nb][j]);
  __syncthreads();
  {
    const int t0 = tid * 4;
    u16 yy[8][4], kk[8][4];
#pragma unroll
    for (int c = 0; c < 8; ++c) {
      uint2 a = *reinterpret_cast<const uint2*>(&hr[c][0][t0]);
      uint2 b = *reinterpret_cast<const uint2*>(&kvT[c][t0]);
      yy[c][0] = a.x & 0xffff; yy[c][1] = a.x >> 16; yy[c][2] = a.y & 0xffff; yy[c][3] = a.y >> 16;
      kk[c][0] = b.x & 0xffff; kk[c][1] = b.x >> 16; kk[c][2] = b.y & 0xffff; kk[c][3] = b.y >> 16;
    }
#pragma unroll
    for (int r = 0; r < 4; ++r) {
      uint4 u = *reinterpret_cast<const uint4*>(q + (size_t)(t0 + r) * DM + c0);
      unsigned ww[4] = {u.x, u.y, u.z, u.w}, ow[4];
      u16 res[8];
#pragma unroll
      for (int c = 0; c < 8; ++c) {
        float qf = (c & 1) ? __uint_as_float(ww[c >> 1] & 0xffff0000u) : __uint_as_float(ww[c >> 1] << 16);
        float y2 = rbf(bf2f(yy[c][r]) + rbf(bf2f(kk[c][r]) * bf2f(ssm_D[c0 + c])));
        res[c] = f2bf(qf * y2);
      }
#pragma unroll
      for (int j = 0; j < 4; ++j) ow[j] = res[2 * j] | ((unsigned)res[2 * j + 1] << 16);
      *reinterpret_cast<uint4*>(out + (size_t)(t0 + r) * (2 * DM) + DM + c0) = make_uint4(ow[0], ow[1], ow[2], ow[3]);
    }
  }
}

extern "C" void kernel_launch(void* const* d_in, const int* in_sizes, int n_in, void* d_out, int out_size,
                              void* d_ws, size_t ws_size, hipStream_t stream) {
  const u16* x = (const u16*)d_in[0];
  const u16* wqkv = (const u16*)d_in[1];
  const u16* wo = (const u16*)d_in[2];
  u16* out = (u16*)d_out;
  char* ws = (char*)d_ws;
  u16* pq = (u16*)ws;
  u16* o = (u16*)(ws + (4u << 20));
  float* part = (float*)(ws + (8u << 20));
  float* zpart = (float*)(ws + (10u << 20));
  k_conv<<<128, 512, 0, stream>>>((const u16*)d_in[3], (const u16*)d_in[4], (const u16*)d_in[5],
                                  (const u16*)d_in[6], (const u16*)d_in[7], (const u16*)d_in[8], out);
  k_qkv<<<256, 512, 0, stream>>>(x, wqkv, pq, part, zpart);
  k_attn_o<<<256, 256, 0, stream>>>(pq, part, zpart, o);
  k_out<<<256, 256, 0, stream>>>(o, wo, out);
}
```

```cpp
#include <hip/hip_runtime.h>
#include <hip/hip_bf16.h>
#include <stdint.h>

typedef short bf16x8 __attribute__((ext_vector_type(8)));
typedef short s16x4 __attribute__((ext_vector_type(4)));
typedef float f32x4 __attribute__((ext_vector_type(4)));
typedef unsigned short u16;

constexpr int S_LEN = 2048, DM = 1024;

__device__ __forceinline__ float bf2f(u16 v) { return __uint_as_float(((unsigned)v) << 16); }
__device__ __forceinline__ u16 f2bf(float f) {
  __bf16 b = (__bf16)f;
  return __builtin_bit_cast(u16, b);
}
__device__ __forceinline__ float rbf(float f) { return bf2f(f2bf(f)); }
__device__ __forceinline__ float phi(float t) {
  float e = rbf(__expf(fminf(t, 0.f)));
  float l = rbf(t + 1.0f);
  float r = t > 0.f ? l : e;
  return rbf(r + 1e-6f);
}

__device__ __forceinline__ void tile_map(int b, int& rt, int& ct) {
  int x = b & 7, i = b >> 3;
  rt = (x & 1) * 8 + (i & 7);
  ct = (x >> 1) * 4 + (i >> 3);
}

constexpr int BM = 128, BK = 64;
template <int BN, int WN, int ST> struct GemmCfg {
  static constexpr int NW = 2 * WN;
  static constexpr int NT = BN / WN / 16;
  static constexpr int A_BYTES = BM * BK * 2;
  static constexpr int B_BYTES = BK * BN * 2;
  static constexpr int STAGE = A_BYTES + B_BYTES;
  static constexpr int AI = A_BYTES / 1024 / NW;
  static constexpr int BI = B_BYTES / 1024 / NW;
  static constexpr int LDS_BYTES = ST * STAGE;
};
#define WAITV(n) asm volatile("s_waitcnt vmcnt(%0)" ::"n"(n) : "memory")

template <int BN, int WN, int ST, typename ColF>
__device__ __forceinline__ void gemm_core(const u16* __restrict__ A, const u16* __restrict__ W, int ldw,
                                          int row0, ColF colbase, char* lds,
                                          f32x4 (&acc)[4][GemmCfg<BN, WN, ST>::NT]) {
  using C = GemmCfg<BN, WN, ST>;
  constexpr int NT = C::NT, NK = DM / BK;
  const int tid = threadIdx.x, lane = tid & 63, wid = tid >> 6;
  const int wr = wid / WN, wc = wid % WN, fr = lane & 15, g = lane >> 4;
  const u16* asrc[C::AI];
  const u16* bsrc[C::BI];
#pragma unroll
  for (int i = 0; i < C::AI; ++i) {
    int pos = (wid * C::AI + i) * 64 + lane;
    int r = pos >> 3, c = pos & 7;
    asrc[i] = A + (size_t)(row0 + r) * DM + ((c ^ (r & 7)) * 8);
  }
#pragma unroll
  for (int i = 0; i < C::BI; ++i) {
    int pos = (wid * C::BI + i) * 64 + lane;
    int seg = pos >> 9, r = (pos >> 3) & 63, c = pos & 7;
    int f = ((r >> 1) & 1) | (((r >> 3) & 1) << 1);
    int cs = (((c >> 1) ^ f) << 1) | (c & 1);
    bsrc[i] = W + (size_t)r * ldw + colbase(seg) + cs * 8;
  }
  auto issue = [&](int kt) {
    char* sb = lds + (kt % ST) * C::STAGE;
#pragma unroll
    for (int i = 0; i < C::AI; ++i)
      __builtin_amdgcn_global_load_lds((const unsigned*)(asrc[i] + kt * BK),
                                       (__attribute__((address_space(3))) unsigned*)(sb + (wid * C::AI + i) * 1024), 16, 0, 0);
#pragma unroll
    for (int i = 0; i < C::BI; ++i)
      __builtin_amdgcn_global_load_lds((const unsigned*)(bsrc[i] + (size_t)kt * BK * ldw),
                                       (__attribute__((address_space(3))) unsigned*)(sb + C::A_BYTES + (wid * C::BI + i) * 1024), 16, 0, 0);
  };
#pragma unroll
  for (int s = 0; s < ST - 1; ++s) issue(s);
  int aoff[4][2];
#pragma unroll
  for (int m = 0; m < 4; ++m)
#pragma unroll
    for (int ks = 0; ks < 2; ++ks) {
      int r = wr * 64 + m * 16 + fr;
      aoff[m][ks] = r * 128 + (((ks * 4 + g) ^ (r & 7)) * 16);
    }
  int boff[NT];
#pragma unroll
  for (int n = 0; n < NT; ++n) {
    int col = wc * (BN / WN) + n * 16, seg = col >> 6, cp = (col & 63) >> 4;
    int r = 8 * g + (fr >> 2), p = fr & 3;
    int f = ((r >> 1) & 1) | (((r >> 3) & 1) << 1);
    boff[n] = C::A_BYTES + seg * 8192 + r * 128 + ((cp ^ f) * 32) + p * 8;
  }
  constexpr int PER = C::AI + C::BI;
  bf16x8 af0[4], bf0[NT], af1[4], bf1[NT];
  auto rdfrag = [&](const char* sb, int ks, bf16x8 (&af)[4], bf16x8 (&bfr)[NT]) {
#pragma unroll
    for (int n = 0; n < NT; ++n) {
      const char* pb = sb + boff[n] + ks * 32 * 128;
      s16x4 lo = __builtin_amdgcn_ds_read_tr16_b64_v4i16((__attribute__((address_space(3))) s16x4*)pb);
      s16x4 hi = __builtin_amdgcn_ds_read_tr16_b64_v4i16((__attribute__((address_space(3))) s16x4*)(pb + 4 * 128));
      bfr[n] = (bf16x8){lo[0], lo[1], lo[2], lo[3], hi[0], hi[1], hi[2], hi[3]};
    }
#pragma unroll
    for (int m = 0; m < 4; ++m) af[m] = *reinterpret_cast<const bf16x8*>(sb + aoff[m][ks]);
  };
  auto mma = [&](bf16x8 (&af)[4], bf16x8 (&bfr)[NT]) {
#pragma unroll
    for (int m = 0; m < 4; ++m)
#pragma unroll
      for (int n = 0; n < NT; ++n)
        acc[m][n] = __builtin_amdgcn_mfma_f32_16x16x32_bf16(af[m], bfr[n], acc[m][n], 0, 0, 0);
  };
  WAITV(PER * (ST - 2));
  __builtin_amdgcn_s_barrier();
  rdfrag(lds, 0, af0, bf0);
#pragma unroll 1
  for (int kt = 0; kt < NK; ++kt) {
    rdfrag(lds + (kt % ST) * C::STAGE, 1, af1, bf1);
    __builtin_amdgcn_sched_barrier(0);
    mma(af0, bf0);
    __builtin_amdgcn_sched_barrier(0);
    if (kt + 1 < NK) {
      if (kt + ST - 2 < NK) WAITV(PER * (ST - 3)); else WAITV(0);
      __builtin_amdgcn_s_barrier();
      if (kt + ST - 1 < NK) issue(kt + ST - 1);
      rdfrag(lds + ((kt + 1) % ST) * C::STAGE, 0, af0, bf0);
    }
    __builtin_amdgcn_sched_barrier(0);
    mma(af1, bf1);
    __builtin_amdgcn_sched_barrier(0);
  }
  __syncthreads();
}

__global__ __launch_bounds__(512) void k_qkv(const u16* __restrict__ x, const u16* __restrict__ wqkv,
                                             u16* __restrict__ pq, float* __restrict__ part,
                                             float* __restrict__ zpart) {
  using C = GemmCfg<192, 4, 4>;
  __shared__ __attribute__((aligned(16))) char lds[C::LDS_BYTES];
  int rt, p; tile_map(blockIdx.x, rt, p);
  const int row0 = rt * BM;
  f32x4 acc[4][3];
#pragma unroll
  for (int m = 0; m < 4; ++m)
#pragma unroll
    for (int n = 0; n < 3; ++n) acc[m][n] = (f32x4){0.f, 0.f, 0.f, 0.f};
  gemm_core<192, 4, 4>(x, wqkv, 3 * DM, row0, [&](int seg) { return seg * DM + p * 64; }, lds, acc);

  const int lane = threadIdx.x & 63, wid = threadIdx.x >> 6;
  const int wr = wid >> 2, wc = wid & 3, fr = lane & 15, g = lane >> 4;
  constexpr int TS = 136;
  u16* pkT = (u16*)lds;
  u16* vT = pkT + 64 * TS;
  u16* pqs = vT + 64 * TS;
#pragma unroll
  for (int n = 0; n < 3; ++n) {
    const int nt = wc * 3 + n, col = nt * 16 + fr;
#pragma unroll
    for (int m = 0; m < 4; ++m) {
      const int r = wr * 64 + m * 16 + g * 4;
      u16 t[4];
#pragma unroll
      for (int j = 0; j < 4; ++j) {
        float v = rbf(acc[m][n][j]);
        t[j] = f2bf(nt < 8 ? phi(v) : v);
      }
      if (nt < 4) {
#pragma unroll
        for (int j = 0; j < 4; ++j) pqs[(r + j) * 72 + col] = t[j];
      } else {
        u16* dst = (nt < 8 ? pkT + (col - 64) * TS : vT + (col - 128) * TS) + r;
        *reinterpret_cast<uint2*>(dst) = make_uint2(t[0] | ((unsigned)t[1] << 16), t[2] | ((unsigned)t[3] << 16));
      }
    }
  }
  __syncthreads();
#pragma unroll
  for (int i = 0; i < 2; ++i) {
    int c = threadIdx.x + 512 * i, r = c >> 3, ch = c & 7;
    *reinterpret_cast<uint4*>(pq + (size_t)(row0 + r) * DM + p * 64 + ch * 8) =
        *reinterpret_cast<const uint4*>(pqs + r * 72 + ch * 8);
  }
  if (wid < 4) {
    const int hh = wid >> 1;
    f32x4 c0 = {0, 0, 0, 0}, c1 = {0, 0, 0, 0}, cz = {0, 0, 0, 0};
    const short one = (short)0x3F80;
    const bf16x8 ones = {one, one, one, one, one, one, one, one};
#pragma unroll
    for (int ks = 0; ks < 4; ++ks) {
      bf16x8 a = *reinterpret_cast<const bf16x8*>(pkT + (wid * 16 + fr) * TS + ks * 32 + g * 8);
      bf16x8 b0 = *reinterpret_cast<const bf16x8*>(vT + (hh * 32 + fr) * TS + ks * 32 + g * 8);
      bf16x8 b1 = *reinterpret_cast<const bf16x8*>(vT + (hh * 32 + 16 + fr) * TS + ks * 32 + g * 8);
      c0 = __builtin_amdgcn_mfma_f32_16x16x32_bf16(a, b0, c0, 0, 0, 0);
      c1 = __builtin_amdgcn_mfma_f32_16x16x32_bf16(a, b1, c1, 0, 0, 0);
      cz = __builtin_amdgcn_mfma_f32_16x16x32_bf16(a, ones, cz, 0, 0, 0);
    }
    const int head = p * 2 + hh;
    float* pp = part + ((size_t)rt * 32 + head) * 1024;
#pragma unroll
    for (int j = 0; j < 4; ++j) {
      const int d = (wid & 1) * 16 + g * 4 + j;
      pp[d * 32 + fr] = c0[j];
      pp[d * 32 + 16 + fr] = c1[j];
      if (fr == 0) zpart[((size_t)rt * 32 + head) * 32 + d] = cz[j];
    }
  }
}

__global__ __launch_bounds__(256) void k_attn_o(const u16* __restrict__ pq, const float* __restrict__ part,
                                                const float* __restrict__ zpart, u16* __restrict__ o) {
  __shared__ __attribute__((aligned(16))) u16 kvT[2][32][40];
  __shared__ __attribute__((aligned(16))) u16 zs[2][32];
  const int rt = blockIdx.x >> 4, p = blockIdx.x & 15, tid = threadIdx.x;
#pragma unroll
  for (int i = 0; i < 2; ++i) {
    int c = tid + 256 * i;
    int hh = c >> 8, d = (c >> 3) & 31, e4 = (c & 7) * 4;
    float4 s = make_float4(0, 0, 0, 0);
#pragma unroll
    for (int t = 0; t < 16; ++t) {
      float4 v = *reinterpret_cast<const float4*>(part + ((size_t)t * 32 + p * 2 + hh) * 1024 + d * 32 + e4);
      s.x += v.x; s.y += v.y; s.z += v.z; s.w += v.w;
    }
    kvT[hh][e4][d] = f2bf(s.x); kvT[hh][e4 + 1][d] = f2bf(s.y);
    kvT[hh][e4 + 2][d] = f2bf(s.z); kvT[hh][e4 + 3][d] = f2bf(s.w);
  }
  if (tid < 64) {
    float s = 0.f;
#pragma unroll
    for (int t = 0; t < 16; ++t) s += zpart[((size_t)t * 32 + p * 2) * 32 + tid];
    zs[tid >> 5][tid & 31] = f2bf(s);
  }
  __syncthreads();
  const int lane = tid & 63, wid = tid >> 6, fr = lane & 15, g = lane >> 4;
#pragma unroll
  for (int rb = 0; rb < 2; ++rb) {
    const int row = rt * 128 + wid * 32 + rb * 16 + fr;
#pragma unroll
    for (int hh = 0; hh < 2; ++hh) {
      bf16x8 b = *reinterpret_cast<const bf16x8*>(pq + (size_t)row * DM + p * 64 + hh * 32 + g * 8);
      bf16x8 a0 = *reinterpret_cast<const bf16x8*>(&kvT[hh][fr][g * 8]);
      bf16x8 a1 = *reinterpret_cast<const bf16x8*>(&kvT[hh][16 + fr][g * 8]);
      bf16x8 az = *reinterpret_cast<const bf16x8*>(&zs[hh][g * 8]);
      f32x4 z4 = {0, 0, 0, 0};
      f32x4 n0 = __builtin_amdgcn_mfma_f32_16x16x32_bf16(a0, b, z4, 0, 0, 0);
      f32x4 n1 = __builtin_amdgcn_mfma_f32_16x16x32_bf16(a1, b, z4, 0, 0, 0);
      f32x4 dn = __builtin_amdgcn_mfma_f32_16x16x32_bf16(az, b, z4, 0, 0, 0);
      const float den = rbf(rbf(dn[0]) + 1e-6f);
      u16 r0[4], r1[4];
#pragma unroll
      for (int j = 0; j < 4; ++j) { r0[j] = f2bf(rbf(n0[j]) / den); r1[j] = f2bf(rbf(n1[j]) / den); }
      u16* op = o + (size_t)row * DM + p * 64 + hh * 32 + g * 4;
      *reinterpret_cast<uint2*>(op) = make_uint2(r0[0] | ((unsigned)r0[1] << 16), r0[2] | ((unsigned)r0[3] << 16));
      *reinterpret_cast<uint2*>(op + 16) = make_uint2(r1[0] | ((unsigned)r1[1] << 16), r1[2] | ((unsigned)r1[3] << 16));
    }
  }
}

__global__ __launch_bounds__(256) void k_out(const u16* __restrict__ o, const u16* __restrict__ wo,
                                             u16* __restrict__ out) {
  using C = GemmCfg<64, 2, 4>;
  __shared__ __attribute__((aligned(16))) char lds[C::LDS_BYTES];
  int rt, ct; tile_map(blockIdx.x, rt, ct);
  f32x4 acc[4][2];
#pragma unroll
  for (int m = 0; m < 4; ++m)
#pragma unroll
    for (int n = 0; n < 2; ++n) acc[m][n] = (f32x4){0.f, 0.f, 0.f, 0.f};
  gemm_core<64, 2, 4>(o, wo, DM, rt * BM, [&](int) { return ct * 64; }, lds, acc);
  const int lane = threadIdx.x & 63, wid = threadIdx.x >> 6;
  const int wr = wid >> 1, wc = wid & 1, fr = lane & 15, g = lane >> 4;
  u16* os = (u16*)lds;
#pragma unroll
  for (int m = 0; m < 4; ++m)
#pragma unroll
    for (int n = 0; n < 2; ++n)
#pragma unroll
      for (int j = 0; j < 4; ++j)
        os[(wr * 64 + m * 16 + g * 4 + j) * 72 + wc * 32 + n * 16 + fr] = f2bf(acc[m][n][j]);
  __syncthreads();
#pragma unroll
  for (int i = 0; i < 4; ++i) {
    int c = threadIdx.x + 256 * i, r = c >> 3, ch = c & 7;
    *reinterpret_cast<uint4*>(out + (size_t)(rt * BM + r) * (2 * DM) + ct * 64 + ch * 8) =
        *reinterpret_cast<const uint4*>(os + r * 72 + ch * 8);
  }
}

constexpr int HR = 2136;
__global__ __launch_bounds__(512) void k_conv(const u16* __restrict__ q, const u16* __restrict__ k,
                                              const u16* __restrict__ v, const u16* __restrict__ shift_k,
                                              const u16* __restrict__ ssm_k, const u16* __restrict__ ssm_D,
                                              u16* __restrict__ out) {
  __shared__ __attribute__((aligned(16))) u16 kvT[4][S_LEN];
  __shared__ __attribute__((aligned(16))) u16 yT[4][S_LEN];
  __shared__ __attribute__((aligned(16))) u16 hr[4][2][HR];
  __shared__ __attribute__((aligned(16))) u16 zrow[64];
  const int c0 = ((blockIdx.x & 7) * 32 + (blockIdx.x >> 3)) * 4, tid = threadIdx.x, lane = tid & 63, w = tid >> 6;
  const int ch = w & 3, hw = w >> 2;
  if (tid < 64) zrow[tid] = 0;
  {
    const u16* hsrc = ssm_k + (size_t)(c0 + ch) * S_LEN;
    uint4 u[2]; u16 nx[2];
#pragma unroll
    for (int i = 0; i < 2; ++i) {
      int idx0 = (lane + 64 * (2 * hw + i)) * 8;
      u[i] = *reinterpret_cast<const uint4*>(hsrc + idx0);
      nx[i] = (idx0 + 8 < S_LEN) ? hsrc[idx0 + 8] : (u16)0;
    }
    if (hw == 0) {
      if (lane < 16) { hr[ch][0][lane] = 0; hr[ch][1][lane] = 0; }
      hr[ch][0][2064 + lane] = 0; hr[ch][1][2064 + lane] = lane == 0 ? hsrc[0] : (u16)0;
      if (lane < HR - 2128) { hr[ch][0][2128 + lane] = 0; hr[ch][1][2128 + lane] = 0; }
    }
#pragma unroll
    for (int i = 0; i < 2; ++i) {
      int idx0 = (lane + 64 * (2 * hw + i)) * 8;
      auto rev = [](unsigned a) { return (a >> 16) | (a << 16); };
      uint4 r0 = make_uint4(rev(u[i].w), rev(u[i].z), rev(u[i].y), rev(u[i].x));
      *reinterpret_cast<uint4*>(&hr[ch][0][2056 - idx0]) = r0;
      uint4 r1 = make_uint4((r0.x << 16) | nx[i], (r0.y << 16) | (r0.x >> 16), (r0.z << 16) | (r0.y >> 16),
                            (r0.w << 16) | (r0.z >> 16));
      *reinterpret_cast<uint4*>(&hr[ch][1][2056 - idx0]) = r1;
    }
  }
  {
    const int t0 = tid * 4;
    uint2 ku[7], vu[4];
#pragma unroll
    for (int i = 0; i < 7; ++i) {
      int t = t0 - 3 + i;
      ku[i] = make_uint2(0, 0);
      if (t >= 0) ku[i] = *reinterpret_cast<const uint2*>(k + (size_t)t * DM + c0);
    }
#pragma unroll
    for (int r = 0; r < 4; ++r) vu[r] = *reinterpret_cast<const uint2*>(v + (size_t)(t0 + r) * DM + c0);
    auto get = [](uint2 u, int c) {
      unsigned x = (c & 2) ? u.y : u.x;
      return (c & 1) ? __uint_as_float(x & 0xffff0000u) : __uint_as_float(x << 16);
    };
#pragma unroll
    for (int c = 0; c < 4; ++c) {
      float wt[4];
#pragma unroll
      for (int t = 0; t < 4; ++t) wt[t] = bf2f(shift_k[(c0 + c) * 4 + 3 - t]);
      u16 res[4];
#pragma unroll
      for (int r = 0; r < 4; ++r) {
        float ks = 0.f;
#pragma unroll
        for (int t = 0; t < 4; ++t) ks = rbf(ks + rbf(get(ku[r + t], c) * wt[t]));
        res[r] = f2bf(ks * get(vu[r], c));
      }
      *reinterpret_cast<uint2*>(&kvT[c][t0]) =
          make_uint2(res[0] | ((unsigned)res[1] << 16), res[2] | ((unsigned)res[3] << 16));
    }
  }
  __syncthreads();
  const int n = lane & 15, g = lane >> 4;
  f32x4 acc[2][2];
#pragma unroll
  for (int a = 0; a < 2; ++a)
#pragma unroll
    for (int b = 0; b < 2; ++b) acc[a][b] = (f32x4){0.f, 0.f, 0.f, 0.f};
  const u16* hbase = &hr[ch][(n & 1) ^ 1][2064 - n - (n & 1) + 8 * g - 32 * hw + 32];
  auto ldf = [&](int jj, bf16x8 (&bfr)[4], bf16x8 (&af)[2][2]) {
#pragma unroll
    for (int f = 0; f < 4; ++f) {
      const unsigned* hp = reinterpret_cast<const unsigned*>(hbase - (64 * jj + 16 * f));
      uint4 u = make_uint4(hp[0], hp[1], hp[2], hp[3]);
      bfr[f] = *reinterpret_cast<bf16x8*>(&u);
    }
#pragma unroll
    for (int ab = 0; ab < 2; ++ab) {
      const int a = ab * 16 + n - jj;
#pragma unroll
      for (int kb = 0; kb < 2; ++kb) {
        const u16* ap = (a >= 0) ? &kvT[ch][a * 64 + kb * 32 + g * 8] : &zrow[g * 8];
        af[ab][kb] = *reinterpret_cast<const bf16x8*>(ap);
      }
    }
  };
  auto mm = [&](int jj, bf16x8 (&bfr)[4], bf16x8 (&af)[2][2]) {
#pragma unroll
    for (int kb = 0; kb < 2; ++kb)
#pragma unroll
      for (int nb = 0; nb < 2; ++nb) {
        if (jj < 16) acc[0][nb] = __builtin_amdgcn_mfma_f32_16x16x32_bf16(af[0][kb], bfr[nb - 2 * kb + 2], acc[0][nb], 0, 0, 0);
        acc[1][nb] = __builtin_amdgcn_mfma_f32_16x16x32_bf16(af[1][kb], bfr[nb - 2 * kb + 2], acc[1][nb], 0, 0, 0);
      }
  };
  bf16x8 b0[4], a0[2][2], b1[4], a1[2][2];
  ldf(0, b0, a0);
#pragma unroll 1
  for (int jj = 0; jj < 32; jj += 2) {
    ldf(jj + 1, b1, a1);
    __builtin_amdgcn_sched_barrier(0);
    mm(jj, b0, a0);
    __builtin_amdgcn_sched_barrier(0);
    if (jj + 2 < 32) ldf(jj + 2, b0, a0);
    __builtin_amdgcn_sched_barrier(0);
    mm(jj + 1, b1, a1);
    __builtin_amdgcn_sched_barrier(0);
  }
#pragma unroll
  for (int ab = 0; ab < 2; ++ab)
#pragma unroll
    for (int nb = 0; nb < 2; ++nb)
#pragma unroll
      for (int j = 0; j < 4; ++j) yT[ch][(ab * 16 + g * 4 + j) * 64 + (hw * 2 + nb) * 16 + n] = f2bf(acc[ab][nb][j]);
  {
    const int t0 = tid * 4;
    uint2 qu[4];
#pragma unroll
    for (int r = 0; r < 4; ++r) qu[r] = *reinterpret_cast<const uint2*>(q + (size_t)(t0 + r) * DM + c0);
    __syncthreads();
    u16 res[4][4];
#pragma unroll
    for (int c = 0; c < 4; ++c) {
      uint2 a = *reinterpret_cast<const uint2*>(&yT[c][t0]);
      uint2 b = *reinterpret_cast<const uint2*>(&kvT[c][t0]);
      const float D = bf2f(ssm_D[c0 + c]);
#pragma unroll
      for (int r = 0; r < 4; ++r) {
        unsigned ya = (r & 2) ? a.y : a.x, kb = (r & 2) ? b.y : b.x, qx = (c & 2) ? qu[r].y : qu[r].x;
        float yf = (r & 1) ? __uint_as_float(ya & 0xffff0000u) : __uint_as_float(ya << 16);
        float kf = (r & 1) ? __uint_as_float(kb & 0xffff0000u) : __uint_as_float(kb << 16);
        float qf = (c & 1) ? __uint_as_float(qx & 0xffff0000u) : __uint_as_float(qx << 16);
        res[r][c] = f2bf(qf * rbf(yf + rbf(kf * D)));
      }
    }
#pragma unroll
    for (int r = 0; r < 4; ++r)
      *reinterpret_cast<uint2*>(out + (size_t)(t0 + r) * (2 * DM) + DM + c0) =
          make_uint2(res[r][0] | ((unsigned)res[r][1] << 16), res[r][2] | ((unsigned)res[r][3] << 16));
  }
}

extern "C" void kernel_launch(void* const* d_in, const int* in_sizes, int n_in, void* d_out, int out_size,
                              void* d_ws, size_t ws_size, hipStream_t stream) {
  const u16* x = (const u16*)d_in[0];
  const u16* wqkv = (const u16*)d_in[1];
  const u16* wo = (const u16*)d_in[2];
  u16* out = (u16*)d_out;
  char* ws = (char*)d_ws;
  u16* pq = (u16*)ws;
  u16* o = (u16*)(ws + (4u << 20));
  float* part = (float*)(ws + (8u << 20));
  float* zpart = (float*)(ws + (10u << 20));
  k_conv<<<256, 512, 0, stream>>>((const u16*)d_in[3], (const u16*)d_in[4], (const u16*)d_in[5],
                                  (const u16*)d_in[6], (const u16*)d_in[7], (const u16*)d_in[8], out);
  k_qkv<<<256, 512, 0, stream>>>(x, wqkv, pq, part, zpart);
  k_attn_o<<<256, 256, 0, stream>>>(pq, part, zpart, o);
  k_out<<<256, 256, 0, stream>>>(o, wo, out);
}
```

```cpp
#include <hip/hip_runtime.h>
#include <hip/hip_bf16.h>
#include <stdint.h>

typedef short bf16x8 __attribute__((ext_vector_type(8)));
typedef short s16x4 __attribute__((ext_vector_type(4)));
typedef float f32x4 __attribute__((ext_vector_type(4)));
typedef unsigned short u16;

constexpr int S_LEN = 2048, DM = 1024;

__device__ __forceinline__ float bf2f(u16 v) { return __uint_as_float(((unsigned)v) << 16); }
__device__ __forceinline__ u16 f2bf(float f) {
  __bf16 b = (__bf16)f;
  return __builtin_bit_cast(u16, b);
}
__device__ __forceinline__ float rbf(float f) { return bf2f(f2bf(f)); }
__device__ __forceinline__ float phi(float t) {
  float e = rbf(__expf(fminf(t, 0.f)));
  float l = rbf(t + 1.0f);
  float r = t > 0.f ? l : e;
  return rbf(r + 1e-6f);
}

__device__ __forceinline__ void tile_map(int b, int& rt, int& ct) {
  int x = b & 7, i = b >> 3;
  rt = (x & 1) * 8 + (i & 7);
  ct = (x >> 1) * 4 + (i >> 3);
}

constexpr int BM = 128, BK = 64;
template <int BN, int WN, int ST> struct GemmCfg {
  static constexpr int NW = 2 * WN;
  static constexpr int NT = BN / WN / 16;
  static constexpr int A_BYTES = BM * BK * 2;
  static constexpr int B_BYTES = BK * BN * 2;
  static constexpr int STAGE = A_BYTES + B_BYTES;
  static constexpr int AI = A_BYTES / 1024 / NW;
  static constexpr int BI = B_BYTES / 1024 / NW;
  static constexpr int LDS_BYTES = ST * STAGE;
};
#define WAITV(n) asm volatile("s_waitcnt vmcnt(%0)" ::"n"(n) : "memory")

template <int BN, int WN, int ST, typename ColF>
__device__ __forceinline__ void gemm_core(const u16* __restrict__ A, const u16* __restrict__ W, int ldw,
                                          int row0, ColF colbase, char* lds,
                                          f32x4 (&acc)[4][GemmCfg<BN, WN, ST>::NT]) {
  using C = GemmCfg<BN, WN, ST>;
  constexpr int NT = C::NT, NK = DM / BK;
  const int tid = threadIdx.x, lane = tid & 63, wid = tid >> 6;
  const int wr = wid / WN, wc = wid % WN, fr = lane & 15, g = lane >> 4;
  const u16* asrc[C::AI];
  const u16* bsrc[C::BI];
#pragma unroll
  for (int i = 0; i < C::AI; ++i) {
    int pos = (wid * C::AI + i) * 64 + lane;
    int r = pos >> 3, c = pos & 7;
    asrc[i] = A + (size_t)(row0 + r) * DM + ((c ^ (r & 7)) * 8);
  }
#pragma unroll
  for (int i = 0; i < C::BI; ++i) {
    int pos = (wid * C::BI + i) * 64 + lane;
    int seg = pos >> 9, r = (pos >> 3) & 63, c = pos & 7;
    int f = ((r >> 1) & 1) | (((r >> 3) & 1) << 1);
    int cs = (((c >> 1) ^ f) << 1) | (c & 1);
    bsrc[i] = W + (size_t)r * ldw + colbase(seg) + cs * 8;
  }
  auto issue = [&](int kt) {
    char* sb = lds + (kt % ST) * C::STAGE;
#pragma unroll
    for (int i = 0; i < C::AI; ++i)
      __builtin_amdgcn_global_load_lds((const unsigned*)(asrc[i] + kt * BK),
                                       (__attribute__((address_space(3))) unsigned*)(sb + (wid * C::AI + i) * 1024), 16, 0, 0);
#pragma unroll
    for (int i = 0; i < C::BI; ++i)
      __builtin_amdgcn_global_load_lds((const unsigned*)(bsrc[i] + (size_t)kt * BK * ldw),
                                       (__attribute__((address_space(3))) unsigned*)(sb + C::A_BYTES + (wid * C::BI + i) * 1024), 16, 0, 0);
  };
#pragma unroll
  for (int s = 0; s < ST - 1; ++s) issue(s);
  int aoff[4][2];
#pragma unroll
  for (int m = 0; m < 4; ++m)
#pragma unroll
    for (int ks = 0; ks < 2; ++ks) {
      int r = wr * 64 + m * 16 + fr;
      aoff[m][ks] = r * 128 + (((ks * 4 + g) ^ (r & 7)) * 16);
    }
  int boff[NT];
#pragma unroll
  for (int n = 0; n < NT; ++n) {
    int col = wc * (BN / WN) + n * 16, seg = col >> 6, cp = (col & 63) >> 4;
    int r = 8 * g + (fr >> 2), p = fr & 3;
    int f = ((r >> 1) & 1) | (((r >> 3) & 1) << 1);
    boff[n] = C::A_BYTES + seg * 8192 + r * 128 + ((cp ^ f) * 32) + p * 8;
  }
  constexpr int PER = C::AI + C::BI;
  bf16x8 af0[4], bf0[NT], af1[4], bf1[NT];
  auto rdfrag = [&](const char* sb, int ks, bf16x8 (&af)[4], bf16x8 (&bfr)[NT]) {
#pragma unroll
    for (int n = 0; n < NT; ++n) {
      const char* pb = sb + boff[n] + ks * 32 * 128;
      s16x4 lo = __builtin_amdgcn_ds_read_tr16_b64_v4i16((__attribute__((address_space(3))) s16x4*)pb);
      s16x4 hi = __builtin_amdgcn_ds_read_tr16_b64_v4i16((__attribute__((address_space(3))) s16x4*)(pb + 4 * 128));
      bfr[n] = (bf16x8){lo[0], lo[1], lo[2], lo[3], hi[0], hi[1], hi[2], hi[3]};
    }
#pragma unroll
    for (int m = 0; m < 4; ++m) af[m] = *reinterpret_cast<const bf16x8*>(sb + aoff[m][ks]);
  };
  auto mma = [&](bf16x8 (&af)[4], bf16x8 (&bfr)[NT]) {
#pragma unroll
    for (int m = 0; m < 4; ++m)
#pragma unroll
      for (int n = 0; n < NT; ++n)
        acc[m][n] = __builtin_amdgcn_mfma_f32_16x16x32_bf16(af[m], bfr[n], acc[m][n], 0, 0, 0);
  };
  WAITV(PER * (ST - 2));
  __builtin_amdgcn_s_barrier();
  rdfrag(lds, 0, af0, bf0);
#pragma unroll 1
  for (int kt = 0; kt < NK; ++kt) {
    rdfrag(lds + (kt % ST) * C::STAGE, 1, af1, bf1);
    __builtin_amdgcn_sched_barrier(0);
    mma(af0, bf0);
    __builtin_amdgcn_sched_barrier(0);
    if (kt + 1 < NK) {
      if (kt + ST - 2 < NK) WAITV(PER * (ST - 3)); else WAITV(0);
      __builtin_amdgcn_s_barrier();
      if (kt + ST - 1 < NK) issue(kt + ST - 1);
      rdfrag(lds + ((kt + 1) % ST) * C::STAGE, 0, af0, bf0);
    }
    __builtin_amdgcn_sched_barrier(0);
    mma(af1, bf1);
    __builtin_amdgcn_sched_barrier(0);
  }
  __syncthreads();
}

__global__ __launch_bounds__(512) void k_qkv(const u16* __restrict__ x, const u16* __restrict__ wqkv,
                                             u16* __restrict__ pq, float* __restrict__ part,
                                             float* __restrict__ zpart) {
  using C = GemmCfg<192, 4, 4>;
  __shared__ __attribute__((aligned(16))) char lds[C::LDS_BYTES];
  int rt, p; tile_map(blockIdx.x, rt, p);
  const int row0 = rt * BM;
  f32x4 acc[4][3];
#pragma unroll
  for (int m = 0; m < 4; ++m)
#pragma unroll
    for (int n = 0; n < 3; ++n) acc[m][n] = (f32x4){0.f, 0.f, 0.f, 0.f};
  gemm_core<192, 4, 4>(x, wqkv, 3 * DM, row0, [&](int seg) { return seg * DM + p * 64; }, lds, acc);

  const int lane = threadIdx.x & 63, wid = threadIdx.x >> 6;
  const int wr = wid >> 2, wc = wid & 3, fr = lane & 15, g = lane >> 4;
  constexpr int TS = 136;
  u16* pkT = (u16*)lds;
  u16* vT = pkT + 64 * TS;
  u16* pqs = vT + 64 * TS;
#pragma unroll
  for (int n = 0; n < 3; ++n) {
    const int nt = wc * 3 + n, col = nt * 16 + fr;
#pragma unroll
    for (int m = 0; m < 4; ++m) {
      const int r = wr * 64 + m * 16 + g * 4;
      u16 t[4];
#pragma unroll
      for (int j = 0; j < 4; ++j) {
        float v = rbf(acc[m][n][j]);
        t[j] = f2bf(nt < 8 ? phi(v) : v);
      }
      if (nt < 4) {
#pragma unroll
        for (int j = 0; j < 4; ++j) pqs[(r + j) * 72 + col] = t[j];
      } else {
        u16* dst = (nt < 8 ? pkT + (col - 64) * TS : vT + (col - 128) * TS) + r;
        *reinterpret_cast<uint2*>(dst) = make_uint2(t[0] | ((unsigned)t[1] << 16), t[2] | ((unsigned)t[3] << 16));
      }
    }
  }
  __syncthreads();
#pragma unroll
  for (int i = 0; i < 2; ++i) {
    int c = threadIdx.x + 512 * i, r = c >> 3, ch = c & 7;
    *reinterpret_cast<uint4*>(pq + (size_t)(row0 + r) * DM + p * 64 + ch * 8) =
        *reinterpret_cast<const uint4*>(pqs + r * 72 + ch * 8);
  }
  if (wid < 4) {
    const int hh = wid >> 1;
    f32x4 c0 = {0, 0, 0, 0}, c1 = {0, 0, 0, 0}, cz = {0, 0, 0, 0};
    const short one = (short)0x3F80;
    const bf16x8 ones = {one, one, one, one, one, one, one, one};
#pragma unroll
    for (int ks = 0; ks < 4; ++ks) {
      bf16x8 a = *reinterpret_cast<const bf16x8*>(pkT + (wid * 16 + fr) * TS + ks * 32 + g * 8);
      bf16x8 b0 = *reinterpret_cast<const bf16x8*>(vT + (hh * 32 + fr) * TS + ks * 32 + g * 8);
      bf16x8 b1 = *reinterpret_cast<const bf16x8*>(vT + (hh * 32 + 16 + fr) * TS + ks * 32 + g * 8);
      c0 = __builtin_amdgcn_mfma_f32_16x16x32_bf16(a, b0, c0, 0, 0, 0);
      c1 = __builtin_amdgcn_mfma_f32_16x16x32_bf16(a, b1, c1, 0, 0, 0);
      cz = __builtin_amdgcn_mfma_f32_16x16x32_bf16(a, ones, cz, 0, 0, 0);
    }
    const int head = p * 2 + hh;
    float* pp = part + ((size_t)rt * 32 + head) * 1024;
#pragma unroll
    for (int j = 0; j < 4; ++j) {
      const int d = (wid & 1) * 16 + g * 4 + j;
      pp[d * 32 + fr] = c0[j];
      pp[d * 32 + 16 + fr] = c1[j];
      if (fr == 0) zpart[((size_t)rt * 32 + head) * 32 + d] = cz[j];
    }
  }
}

__global__ __launch_bounds__(256) void k_attn_o(const u16* __restrict__ pq, const float* __restrict__ part,
                                                const float* __restrict__ zpart, u16* __restrict__ o) {
  __shared__ __attribute__((aligned(16))) u16 kvT[2][32][40];
  __shared__ __attribute__((aligned(16))) u16 zs[2][32];
  const int rt = blockIdx.x >> 4, p = blockIdx.x & 15, tid = threadIdx.x;
#pragma unroll
  for (int i = 0; i < 2; ++i) {
    int c = tid + 256 * i;
    int hh = c >> 8, d = (c >> 3) & 31, e4 = (c & 7) * 4;
    float4 s = make_float4(0, 0, 0, 0);
#pragma unroll
    for (int t = 0; t < 16; ++t) {
      float4 v = *reinterpret_cast<const float4*>(part + ((size_t)t * 32 + p * 2 + hh) * 1024 + d * 32 + e4);
      s.x += v.x; s.y += v.y; s.z += v.z; s.w += v.w;
    }
    kvT[hh][e4][d] = f2bf(s.x); kvT[hh][e4 + 1][d] = f2bf(s.y);
    kvT[hh][e4 + 2][d] = f2bf(s.z); kvT[hh][e4 + 3][d] = f2bf(s.w);
  }
  if (tid < 64) {
    float s = 0.f;
#pragma unroll
    for (int t = 0; t < 16; ++t) s += zpart[((size_t)t * 32 + p * 2) * 32 + tid];
    zs[tid >> 5][tid & 31] = f2bf(s);
  }
  __syncthreads();
  const int lane = tid & 63, wid = tid >> 6, fr = lane & 15, g = lane >> 4;
#pragma unroll
  for (int rb = 0; rb < 2; ++rb) {
    const int row = rt * 128 + wid * 32 + rb * 16 + fr;
#pragma unroll
    for (int hh = 0; hh < 2; ++hh) {
      bf16x8 b = *reinterpret_cast<const bf16x8*>(pq + (size_t)row * DM + p * 64 + hh * 32 + g * 8);
      bf16x8 a0 = *reinterpret_cast<const bf16x8*>(&kvT[hh][fr][g * 8]);
      bf16x8 a1 = *reinterpret_cast<const bf16x8*>(&kvT[hh][16 + fr][g * 8]);
      bf16x8 az = *reinterpret_cast<const bf16x8*>(&zs[hh][g * 8]);
      f32x4 z4 = {0, 0, 0, 0};
      f32x4 n0 = __builtin_amdgcn_mfma_f32_16x16x32_bf16(a0, b, z4, 0, 0, 0);
      f32x4 n1 = __builtin_amdgcn_mfma_f32_16x16x32_bf16(a1, b, z4, 0, 0, 0);
      f32x4 dn = __builtin_amdgcn_mfma_f32_16x16x32_bf16(az, b, z4, 0, 0, 0);
      const float den = rbf(rbf(dn[0]) + 1e-6f);
      u16 r0[4], r1[4];
#pragma unroll
      for (int j = 0; j < 4; ++j) { r0[j] = f2bf(rbf(n0[j]) / den); r1[j] = f2bf(rbf(n1[j]) / den); }
      u16* op = o + (size_t)row * DM + p * 64 + hh * 32 + g * 4;
      *reinterpret_cast<uint2*>(op) = make_uint2(r0[0] | ((unsigned)r0[1] << 16), r0[2] | ((unsigned)r0[3] << 16));
      *reinterpret_cast<uint2*>(op + 16) = make_uint2(r1[0] | ((unsigned)r1[1] << 16), r1[2] | ((unsigned)r1[3] << 16));
    }
  }
}

__global__ __launch_bounds__(256) void k_out(const u16* __restrict__ o, const u16* __restrict__ wo,
                                             u16* __restrict__ out) {
  using C = GemmCfg<64, 2, 4>;
  __shared__ __attribute__((aligned(16))) char lds[C::LDS_BYTES];
  int rt, ct; tile_map(blockIdx.x, rt, ct);
  f32x4 acc[4][2];
#pragma unroll
  for (int m = 0; m < 4; ++m)
#pragma unroll
    for (int n = 0; n < 2; ++n) acc[m][n] = (f32x4){0.f, 0.f, 0.f, 0.f};
  gemm_core<64, 2, 4>(o, wo, DM, rt * BM, [&](int) { return ct * 64; }, lds, acc);
  const int lane = threadIdx.x & 63, wid = threadIdx.x >> 6;
  const int wr = wid >> 1, wc = wid & 1, fr = lane & 15, g = lane >> 4;
  u16* os = (u16*)lds;
#pragma unroll
  for (int m = 0; m < 4; ++m)
#pragma unroll
    for (int n = 0; n < 2; ++n)
#pragma unroll
      for (int j = 0; j < 4; ++j)
        os[(wr * 64 + m * 16 + g * 4 + j) * 72 + wc * 32 + n * 16 + fr] = f2bf(acc[m][n][j]);
  __syncthreads();
#pragma unroll
  for (int i = 0; i < 4; ++i) {
    int c = threadIdx.x + 256 * i, r = c >> 3, ch = c & 7;
    *reinterpret_cast<uint4*>(out + (size_t)(rt * BM + r) * (2 * DM) + ct * 64 + ch * 8) =
        *reinterpret_cast<const uint4*>(os + r * 72 + ch * 8);
  }
}

constexpr int HR = 2136;
__global__ __launch_bounds__(512) void k_conv(const u16* __restrict__ q, const u16* __restrict__ k,
                                              const u16* __restrict__ v, const u16* __restrict__ shift_k,
                                              const u16* __restrict__ ssm_k, const u16* __restrict__ ssm_D,
                                              u16* __restrict__ out) {
  __shared__ __attribute__((aligned(16))) u16 kvT[4][S_LEN];
  __shared__ __attribute__((aligned(16))) u16 yT[4][S_LEN];
  __shared__ __attribute__((aligned(16))) u16 hr[4][4][HR];
  __shared__ __attribute__((aligned(16))) u16 zrow[64];
  const int c0 = ((blockIdx.x & 7) * 32 + (blockIdx.x >> 3)) * 4, tid = threadIdx.x, lane = tid & 63, w = tid >> 6;
  const int ch = w & 3, hw = w >> 2;
  if (tid < 64) zrow[tid] = 0;
  {
    const u16* hsrc = ssm_k + (size_t)(c0 + ch) * S_LEN;
    uint4 u[2]; uint2 nx[2];
#pragma unroll
    for (int i = 0; i < 2; ++i) {
      int idx0 = (lane + 64 * (2 * hw + i)) * 8;
      u[i] = *reinterpret_cast<const uint4*>(hsrc + idx0);
      nx[i] = (idx0 + 8 < S_LEN) ? *reinterpret_cast<const uint2*>(hsrc + idx0 + 8) : make_uint2(0, 0);
    }
    if (hw == 0) {
#pragma unroll
      for (int sft = 0; sft < 4; ++sft) {
        if (lane < 16) hr[ch][sft][lane] = 0;
        hr[ch][sft][2064 + lane] = (sft - 1 - lane >= 0) ? hsrc[sft - 1 - lane] : (u16)0;
        if (lane < HR - 2128) hr[ch][sft][2128 + lane] = 0;
      }
    }
#pragma unroll
    for (int i = 0; i < 2; ++i) {
      int idx0 = (lane + 64 * (2 * hw + i)) * 8;
      unsigned wv[6] = {u[i].x, u[i].y, u[i].z, u[i].w, nx[i].x, nx[i].y};
      unsigned e[12];
#pragma unroll
      for (int t = 0; t < 12; ++t) e[t] = (t & 1) ? (wv[t >> 1] >> 16) : (wv[t >> 1] & 0xffffu);
#pragma unroll
      for (int sft = 0; sft < 4; ++sft) {
        uint4 r = make_uint4(e[sft + 7] | (e[sft + 6] << 16), e[sft + 5] | (e[sft + 4] << 16),
                             e[sft + 3] | (e[sft + 2] << 16), e[sft + 1] | (e[sft] << 16));
        *reinterpret_cast<uint4*>(&hr[ch][sft][2056 - idx0]) = r;
      }
    }
  }
  {
    const int t0 = tid * 4;
    uint2 ku[7], vu[4];
#pragma unroll
    for (int i = 0; i < 7; ++i) {
      int t = t0 - 3 + i;
      ku[i] = make_uint2(0, 0);
      if (t >= 0) ku[i] = *reinterpret_cast<const uint2*>(k + (size_t)t * DM + c0);
    }
#pragma unroll
    for (int r = 0; r < 4; ++r) vu[r] = *reinterpret_cast<const uint2*>(v + (size_t)(t0 + r) * DM + c0);
    auto get = [](uint2 u, int c) {
      unsigned x = (c & 2) ? u.y : u.x;
      return (c & 1) ? __uint_as_float(x & 0xffff0000u) : __uint_as_float(x << 16);
    };
#pragma unroll
    for (int c = 0; c < 4; ++c) {
      float wt[4];
#pragma unroll
      for (int t = 0; t < 4; ++t) wt[t] = bf2f(shift_k[(c0 + c) * 4 + 3 - t]);
      u16 res[4];
#pragma unroll
      for (int r = 0; r < 4; ++r) {
        float ks = 0.f;
#pragma unroll
        for (int t = 0; t < 4; ++t) ks = rbf(ks + rbf(get(ku[r + t], c) * wt[t]));
        res[r] = f2bf(ks * get(vu[r], c));
      }
      *reinterpret_cast<uint2*>(&kvT[c][t0]) =
          make_uint2(res[0] | ((unsigned)res[1] << 16), res[2] | ((unsigned)res[3] << 16));
    }
  }
  __syncthreads();
  const int n = lane & 15, g = lane >> 4;
  f32x4 acc[2][2];
#pragma unroll
  for (int a = 0; a < 2; ++a)
#pragma unroll
    for (int b = 0; b < 2; ++b) acc[a][b] = (f32x4){0.f, 0.f, 0.f, 0.f};
  const u16* hbase = &hr[ch][(n + 1) & 3][2064 - ((n + 1) & ~3) + 8 * g - 32 * hw + 32];
  const unsigned hoff = (unsigned)(size_t)hbase;
  unsigned hoff2 = hoff + 8;
  asm volatile("" : "+v"(hoff2));
  auto ldf = [&](int jj, bf16x8 (&bfr)[4], bf16x8 (&af)[2][2]) {
#pragma unroll
    for (int f = 0; f < 4; ++f) {
      typedef unsigned u32x2 __attribute__((ext_vector_type(2)));
      typedef const __attribute__((address_space(3))) u32x2* lp2;
      unsigned o0 = hoff - 2 * (64 * jj + 16 * f), o1 = hoff2 - 2 * (64 * jj + 16 * f);
      u32x2 h0 = *(lp2)(size_t)o0, h1 = *(lp2)(size_t)o1;
      uint4 u = make_uint4(h0.x, h0.y, h1.x, h1.y);
      bfr[f] = *reinterpret_cast<bf16x8*>(&u);
    }
#pragma unroll
    for (int ab = 0; ab < 2; ++ab) {
      const int a = ab * 16 + n - jj;
#pragma unroll
      for (int kb = 0; kb < 2; ++kb) {
        const u16* ap = (a >= 0) ? &kvT[ch][a * 64 + kb * 32 + g * 8] : &zrow[g * 8];
        af[ab][kb] = *reinterpret_cast<const bf16x8*>(ap);
      }
    }
  };
  auto mm = [&](int jj, bf16x8 (&bfr)[4], bf16x8 (&af)[2][2]) {
#pragma unroll
    for (int kb = 0; kb < 2; ++kb)
#pragma unroll
      for (int nb = 0; nb < 2; ++nb) {
        if (jj < 16) acc[0][nb] = __builtin_amdgcn_mfma_f32_16x16x32_bf16(af[0][kb], bfr[nb - 2 * kb + 2], acc[0][nb], 0, 0, 0);
        acc[1][nb] = __builtin_amdgcn_mfma_f32_16x16x32_bf16(af[1][kb], bfr[nb - 2 * kb + 2], acc[1][nb], 0, 0, 0);
      }
  };
  bf16x8 b0[4], a0[2][2], b1[4], a1[2][2];
  ldf(0, b0, a0);
#pragma unroll 1
  for (int jj = 0; jj < 32; jj += 2) {
    ldf(jj + 1, b1, a1);
    __builtin_amdgcn_sched_barrier(0);
    mm(jj, b0, a0);
    __builtin_amdgcn_sched_barrier(0);
    if (jj + 2 < 32) ldf(jj + 2, b0, a0);
    __builtin_amdgcn_sched_barrier(0);
    mm(jj + 1, b1, a1);
    __builtin_amdgcn_sched_barrier(0);
  }
#pragma unroll
  for (int ab = 0; ab < 2; ++ab)
#pragma unroll
    for (int nb = 0; nb < 2; ++nb)
#pragma unroll
      for (int j = 0; j < 4; ++j) yT[ch][(ab * 16 + g * 4 + j) * 64 + (hw * 2 + nb) * 16 + n] = f2bf(acc[ab][nb][j]);
  {
    const int t0 = tid * 4;
    uint2 qu[4];
#pragma unroll
    for (int r = 0; r < 4; ++r) qu[r] = *reinterpret_cast<const uint2*>(q + (size_t)(t0 + r) * DM + c0);
    __syncthreads();
    u16 res[4][4];
#pragma unroll
    for (int c = 0; c < 4; ++c) {
      uint2 a = *reinterpret_cast<const uint2*>(&yT[c][t0]);
      uint2 b = *reinterpret_cast<const uint2*>(&kvT[c][t0]);
      const float D = bf2f(ssm_D[c0 + c]);
#pragma unroll
      for (int r = 0; r < 4; ++r) {
        unsigned ya = (r & 2) ? a.y : a.x, kb = (r & 2) ? b.y : b.x, qx = (c & 2) ? qu[r].y : qu[r].x;
        float yf = (r & 1) ? __uint_as_float(ya & 0xffff0000u) : __uint_as_float(ya << 16);
        float kf = (r & 1) ? __uint_as_float(kb & 0xffff0000u) : __uint_as_float(kb << 16);
        float qf = (c & 1) ? __uint_as_float(qx & 0xffff0000u) : __uint_as_float(qx << 16);
        res[r][c] = f2bf(qf * rbf(yf + rbf(kf * D)));
      }
    }
#pragma unroll
    for (int r = 0; r < 4; ++r)
      *reinterpret_cast<uint2*>(out + (size_t)(t0 + r) * (2 * DM) + DM + c0) =
          make_uint2(res[r][0] | ((unsigned)res[r][1] << 16), res[r][2] | ((unsigned)res[r][3] << 16));
  }
}

extern "C" void kernel_launch(void* const* d_in, const int* in_sizes, int n_in, void* d_out, int out_size,
                              void* d_ws, size_t ws_size, hipStream_t stream) {
  const u16* x = (const u16*)d_in[0];
  const u16* wqkv = (const u16*)d_in[1];
  const u16* wo = (const u16*)d_in[2];
  u16* out = (u16*)d_out;
  char* ws = (char*)d_ws;
  u16* pq = (u16*)ws;
  u16* o = (u16*)(ws + (4u << 20));
  float* part = (float*)(ws + (8u << 20));
  float* zpart = (float*)(ws + (10u << 20));
  k_conv<<<256, 512, 0, stream>>>((const u16*)d_in[3], (const u16*)d_in[4], (const u16*)d_in[5],
                                  (const u16*)d_in[6], (const u16*)d_in[7], (const u16*)d_in[8], out);
  k_qkv<<<256, 512, 0, stream>>>(x, wqkv, pq, part, zpart);
  k_attn_o<<<256, 256, 0, stream>>>(pq, part, zpart, o);
  k_out<<<256, 256, 0, stream>>>(o, wo, out);
}
```

```cpp
#include <hip/hip_runtime.h>
#include <hip/hip_bf16.h>
#include <stdint.h>

typedef short bf16x8 __attribute__((ext_vector_type(8)));
typedef short s16x4 __attribute__((ext_vector_type(4)));
typedef float f32x4 __attribute__((ext_vector_type(4)));
typedef unsigned short u16;

constexpr int S_LEN = 2048, DM = 1024;

__device__ __forceinline__ float bf2f(u16 v) { return __uint_as_float(((unsigned)v) << 16); }
__device__ __forceinline__ u16 f2bf(float f) {
  __bf16 b = (__bf16)f;
  return __builtin_bit_cast(u16, b);
}
__device__ __forceinline__ float rbf(float f) { return bf2f(f2bf(f)); }
__device__ __forceinline__ float phi(float t) {
  float e = rbf(__expf(fminf(t, 0.f)));
  float l = rbf(t + 1.0f);
  float r = t > 0.f ? l : e;
  return rbf(r + 1e-6f);
}

__device__ __forceinline__ void tile_map(int b, int& rt, int& ct) {
  int x = b & 7, i = b >> 3;
  rt = (x & 1) * 8 + (i & 7);
  ct = (x >> 1) * 4 + (i >> 3);
}

constexpr int BM = 128, BK = 64;
template <int BN, int WN, int ST> struct GemmCfg {
  static constexpr int NW = 2 * WN;
  static constexpr int NT = BN / WN / 16;
  static constexpr int A_BYTES = BM * BK * 2;
  static constexpr int B_BYTES = BK * BN * 2;
  static constexpr int STAGE = A_BYTES + B_BYTES;
  static constexpr int AI = A_BYTES / 1024 / NW;
  static constexpr int BI = B_BYTES / 1024 / NW;
  static constexpr int LDS_BYTES = ST * STAGE;
};
#define WAITV(n) asm volatile("s_waitcnt vmcnt(%0)" ::"n"(n) : "memory")

template <int BN, int WN, int ST, typename ColF>
__device__ __forceinline__ void gemm_core(const u16* __restrict__ A, const u16* __restrict__ W, int ldw,
                                          int row0, ColF colbase, char* lds,
                                          f32x4 (&acc)[4][GemmCfg<BN, WN, ST>::NT]) {
  using C = GemmCfg<BN, WN, ST>;
  constexpr int NT = C::NT, NK = DM / BK;
  const int tid = threadIdx.x, lane = tid & 63, wid = tid >> 6;
  const int wr = wid / WN, wc = wid % WN, fr = lane & 15, g = lane >> 4;
  const u16* asrc[C::AI];
  const u16* bsrc[C::BI];
#pragma unroll
  for (int i = 0; i < C::AI; ++i) {
    int pos = (wid * C::AI + i) * 64 + lane;
    int r = pos >> 3, c = pos & 7;
    asrc[i] = A + (size_t)(row0 + r) * DM + ((c ^ (r & 7)) * 8);
  }
#pragma unroll
  for (int i = 0; i < C::BI; ++i) {
    int pos = (wid * C::BI + i) * 64 + lane;
    int seg = pos >> 9, r = (pos >> 3) & 63, c = pos & 7;
    int f = ((r >> 1) & 1) | (((r >> 3) & 1) << 1);
    int cs = (((c >> 1) ^ f) << 1) | (c & 1);
    bsrc[i] = W + (size_t)r * ldw + colbase(seg) + cs * 8;
  }
  auto issue = [&](int kt) {
    char* sb = lds + (kt % ST) * C::STAGE;
#pragma unroll
    for (int i = 0; i < C::AI; ++i)
      __builtin_amdgcn_global_load_lds((const unsigned*)(asrc[i] + kt * BK),
                                       (__attribute__((address_space(3))) unsigned*)(sb + (wid * C::AI + i) * 1024), 16, 0, 0);
#pragma unroll
    for (int i = 0; i < C::BI; ++i)
      __builtin_amdgcn_global_load_lds((const unsigned*)(bsrc[i] + (size_t)kt * BK * ldw),
                                       (__attribute__((address_space(3))) unsigned*)(sb + C::A_BYTES + (wid * C::BI + i) * 1024), 16, 0, 0);
  };
#pragma unroll
  for (int s = 0; s < ST - 1; ++s) issue(s);
  int aoff[4][2];
#pragma unroll
  for (int m = 0; m < 4; ++m)
#pragma unroll
    for (int ks = 0; ks < 2; ++ks) {
      int r = wr * 64 + m * 16 + fr;
      aoff[m][ks] = r * 128 + (((ks * 4 + g) ^ (r & 7)) * 16);
    }
  int boff[NT];
#pragma unroll
  for (int n = 0; n < NT; ++n) {
    int col = wc * (BN / WN) + n * 16, seg = col >> 6, cp = (col & 63) >> 4;
    int r = 8 * g + (fr >> 2), p = fr & 3;
    int f = ((r >> 1) & 1) | (((r >> 3) & 1) << 1);
    boff[n] = C::A_BYTES + seg * 8192 + r * 128 + ((cp ^ f) * 32) + p * 8;
  }
  constexpr int PER = C::AI + C::BI;
  bf16x8 af0[4], bf0[NT], af1[4], bf1[NT];
  auto rdfrag = [&](const char* sb, int ks, bf16x8 (&af)[4], bf16x8 (&bfr)[NT]) {
#pragma unroll
    for (int n = 0; n < NT; ++n) {
      const char* pb = sb + boff[n] + ks * 32 * 128;
      s16x4 lo = __builtin_amdgcn_ds_read_tr16_b64_v4i16((__attribute__((address_space(3))) s16x4*)pb);
      s16x4 hi = __builtin_amdgcn_ds_read_tr16_b64_v4i16((__attribute__((address_space(3))) s16x4*)(pb + 4 * 128));
      bfr[n] = (bf16x8){lo[0], lo[1], lo[2], lo[3], hi[0], hi[1], hi[2], hi[3]};
    }
#pragma unroll
    for (int m = 0; m < 4; ++m) af[m] = *reinterpret_cast<const bf16x8*>(sb + aoff[m][ks]);
  };
  auto mma = [&](bf16x8 (&af)[4], bf16x8 (&bfr)[NT]) {
#pragma unroll
    for (int m = 0; m < 4; ++m)
#pragma unroll
      for (int n = 0; n < NT; ++n)
        acc[m][n] = __builtin_amdgcn_mfma_f32_16x16x32_bf16(af[m], bfr[n], acc[m][n], 0, 0, 0);
  };
  WAITV(PER * (ST - 2));
  __builtin_amdgcn_s_barrier();
  rdfrag(lds, 0, af0, bf0);
#pragma unroll 1
  for (int kt = 0; kt < NK; ++kt) {
    rdfrag(lds + (kt % ST) * C::STAGE, 1, af1, bf1);
    __builtin_amdgcn_sched_barrier(0);
    mma(af0, bf0);
    __builtin_amdgcn_sched_barrier(0);
    if (kt + 1 < NK) {
      if (kt + ST - 2 < NK) WAITV(PER * (ST - 3)); else WAITV(0);
      __builtin_amdgcn_s_barrier();
      if (kt + ST - 1 < NK) issue(kt + ST - 1);
      rdfrag(lds + ((kt + 1) % ST) * C::STAGE, 0, af0, bf0);
    }
    __builtin_amdgcn_sched_barrier(0);
    mma(af1, bf1);
    __builtin_amdgcn_sched_barrier(0);
  }
  __syncthreads();
}

__global__ __launch_bounds__(512) void k_qkv(const u16* __restrict__ x, const u16* __restrict__ wqkv,
                                             u16* __restrict__ pq, float* __restrict__ part,
                                             float* __restrict__ zpart) {
  using C = GemmCfg<192, 4, 4>;
  __shared__ __attribute__((aligned(16))) char lds[C::LDS_BYTES];
  int rt, p; tile_map(blockIdx.x, rt, p);
  const int row0 = rt * BM;
  f32x4 acc[4][3];
#pragma unroll
  for (int m = 0; m < 4; ++m)
#pragma unroll
    for (int n = 0; n < 3; ++n) acc[m][n] = (f32x4){0.f, 0.f, 0.f, 0.f};
  gemm_core<192, 4, 4>(x, wqkv, 3 * DM, row0, [&](int seg) { return seg * DM + p * 64; }, lds, acc);

  const int lane = threadIdx.x & 63, wid = threadIdx.x >> 6;
  const int wr = wid >> 2, wc = wid & 3, fr = lane & 15, g = lane >> 4;
  constexpr int TS = 136;
  u16* pkT = (u16*)lds;
  u16* vT = pkT + 64 * TS;
  u16* pqs = vT + 64 * TS;
#pragma unroll
  for (int n = 0; n < 3; ++n) {
    const int nt = wc * 3 + n, col = nt * 16 + fr;
#pragma unroll
    for (int m = 0; m < 4; ++m) {
      const int r = wr * 64 + m * 16 + g * 4;
      u16 t[4];
#pragma unroll
      for (int j = 0; j < 4; ++j) {
        float v = rbf(acc[m][n][j]);
        t[j] = f2bf(nt < 8 ? phi(v) : v);
      }
      if (nt < 4) {
#pragma unroll
        for (int j = 0; j < 4; ++j) pqs[(r + j) * 72 + col] = t[j];
      } else {
        u16* dst = (nt < 8 ? pkT + (col - 64) * TS : vT + (col - 128) * TS) + r;
        *reinterpret_cast<uint2*>(dst) = make_uint2(t[0] | ((unsigned)t[1] << 16), t[2] | ((unsigned)t[3] << 16));
      }
    }
  }
  __syncthreads();
#pragma unroll
  for (int i = 0; i < 2; ++i) {
    int c = threadIdx.x + 512 * i, r = c >> 3, ch = c & 7;
    *reinterpret_cast<uint4*>(pq + (size_t)(row0 + r) * DM + p * 64 + ch * 8) =
        *reinterpret_cast<const uint4*>(pqs + r * 72 + ch * 8);
  }
  if (wid < 4) {
    const int hh = wid >> 1;
    f32x4 c0 = {0, 0, 0, 0}, c1 = {0, 0, 0, 0}, cz = {0, 0, 0, 0};
    const short one = (short)0x3F80;
    const bf16x8 ones = {one, one, one, one, one, one, one, one};
#pragma unroll
    for (int ks = 0; ks < 4; ++ks) {
      bf16x8 a = *reinterpret_cast<const bf16x8*>(pkT + (wid * 16 + fr) * TS + ks * 32 + g * 8);
      bf16x8 b0 = *reinterpret_cast<const bf16x8*>(vT + (hh * 32 + fr) * TS + ks * 32 + g * 8);
      bf16x8 b1 = *reinterpret_cast<const bf16x8*>(vT + (hh * 32 + 16 + fr) * TS + ks * 32 + g * 8);
      c0 = __builtin_amdgcn_mfma_f32_16x16x32_bf16(a, b0, c0, 0, 0, 0);
      c1 = __builtin_amdgcn_mfma_f32_16x16x32_bf16(a, b1, c1, 0, 0, 0);
      cz = __builtin_amdgcn_mfma_f32_16x16x32_bf16(a, ones, cz, 0, 0, 0);
    }
    const int head = p * 2 + hh;
    float* pp = part + ((size_t)rt * 32 + head) * 1024;
#pragma unroll
    for (int j = 0; j < 4; ++j) {
      const int d = (wid & 1) * 16 + g * 4 + j;
      pp[d * 32 + fr] = c0[j];
      pp[d * 32 + 16 + fr] = c1[j];
      if (fr == 0) zpart[((size_t)rt * 32 + head) * 32 + d] = cz[j];
    }
  }
}

__device__ __forceinline__ void attn_o(const u16* __restrict__ pq, const float* __restrict__ part,
                                       const float* __restrict__ zpart, u16* __restrict__ o, int unit, int tid,
                                       u16* sm) {
  u16 (*kvT)[32][40] = reinterpret_cast<u16 (*)[32][40]>(sm);
  u16 (*zs)[32] = reinterpret_cast<u16 (*)[32]>(sm + 2 * 32 * 40);
  const int rt = unit >> 4, p = unit & 15;
#pragma unroll
  for (int i = 0; i < 2; ++i) {
    int c = tid + 256 * i;
    int hh = c >> 8, d = (c >> 3) & 31, e4 = (c & 7) * 4;
    float4 s = make_float4(0, 0, 0, 0);
#pragma unroll
    for (int t = 0; t < 16; ++t) {
      float4 v = *reinterpret_cast<const float4*>(part + ((size_t)t * 32 + p * 2 + hh) * 1024 + d * 32 + e4);
      s.x += v.x; s.y += v.y; s.z += v.z; s.w += v.w;
    }
    kvT[hh][e4][d] = f2bf(s.x); kvT[hh][e4 + 1][d] = f2bf(s.y);
    kvT[hh][e4 + 2][d] = f2bf(s.z); kvT[hh][e4 + 3][d] = f2bf(s.w);
  }
  if (tid < 64) {
    float s = 0.f;
#pragma unroll
    for (int t = 0; t < 16; ++t) s += zpart[((size_t)t * 32 + p * 2) * 32 + tid];
    zs[tid >> 5][tid & 31] = f2bf(s);
  }
  __syncthreads();
  const int lane = tid & 63, wid = tid >> 6, fr = lane & 15, g = lane >> 4;
#pragma unroll
  for (int rb = 0; rb < 2; ++rb) {
    const int row = rt * 128 + wid * 32 + rb * 16 + fr;
#pragma unroll
    for (int hh = 0; hh < 2; ++hh) {
      bf16x8 b = *reinterpret_cast<const bf16x8*>(pq + (size_t)row * DM + p * 64 + hh * 32 + g * 8);
      bf16x8 a0 = *reinterpret_cast<const bf16x8*>(&kvT[hh][fr][g * 8]);
      bf16x8 a1 = *reinterpret_cast<const bf16x8*>(&kvT[hh][16 + fr][g * 8]);
      bf16x8 az = *reinterpret_cast<const bf16x8*>(&zs[hh][g * 8]);
      f32x4 z4 = {0, 0, 0, 0};
      f32x4 n0 = __builtin_amdgcn_mfma_f32_16x16x32_bf16(a0, b, z4, 0, 0, 0);
      f32x4 n1 = __builtin_amdgcn_mfma_f32_16x16x32_bf16(a1, b, z4, 0, 0, 0);
      f32x4 dn = __builtin_amdgcn_mfma_f32_16x16x32_bf16(az, b, z4, 0, 0, 0);
      const float den = rbf(rbf(dn[0]) + 1e-6f);
      u16 r0[4], r1[4];
#pragma unroll
      for (int j = 0; j < 4; ++j) { r0[j] = f2bf(rbf(n0[j]) / den); r1[j] = f2bf(rbf(n1[j]) / den); }
      u16* op = o + (size_t)row * DM + p * 64 + hh * 32 + g * 4;
      *reinterpret_cast<uint2*>(op) = make_uint2(r0[0] | ((unsigned)r0[1] << 16), r0[2] | ((unsigned)r0[3] << 16));
      *reinterpret_cast<uint2*>(op + 16) = make_uint2(r1[0] | ((unsigned)r1[1] << 16), r1[2] | ((unsigned)r1[3] << 16));
    }
  }
}

__global__ __launch_bounds__(256) void k_out(const u16* __restrict__ o, const u16* __restrict__ wo,
                                             u16* __restrict__ out) {
  using C = GemmCfg<64, 2, 4>;
  __shared__ __attribute__((aligned(16))) char lds[C::LDS_BYTES];
  int rt, ct; tile_map(blockIdx.x, rt, ct);
  f32x4 acc[4][2];
#pragma unroll
  for (int m = 0; m < 4; ++m)
#pragma unroll
    for (int n = 0; n < 2; ++n) acc[m][n] = (f32x4){0.f, 0.f, 0.f, 0.f};
  gemm_core<64, 2, 4>(o, wo, DM, rt * BM, [&](int) { return ct * 64; }, lds, acc);
  const int lane = threadIdx.x & 63, wid = threadIdx.x >> 6;
  const int wr = wid >> 1, wc = wid & 1, fr = lane & 15, g = lane >> 4;
  u16* os = (u16*)lds;
#pragma unroll
  for (int m = 0; m < 4; ++m)
#pragma unroll
    for (int n = 0; n < 2; ++n)
#pragma unroll
      for (int j = 0; j < 4; ++j)
        os[(wr * 64 + m * 16 + g * 4 + j) * 72 + wc * 32 + n * 16 + fr] = f2bf(acc[m][n][j]);
  __syncthreads();
#pragma unroll
  for (int i = 0; i < 4; ++i) {
    int c = threadIdx.x + 256 * i, r = c >> 3, ch = c & 7;
    *reinterpret_cast<uint4*>(out + (size_t)(rt * BM + r) * (2 * DM) + ct * 64 + ch * 8) =
        *reinterpret_cast<const uint4*>(os + r * 72 + ch * 8);
  }
}

constexpr int HR = 2136;
constexpr int CONV_LDS = 4 * 32 * 72 + 4 * S_LEN + 8 * HR + 64;
__global__ __launch_bounds__(512) void k_conv(const u16* __restrict__ q, const u16* __restrict__ k,
                                              const u16* __restrict__ v, const u16* __restrict__ shift_k,
                                              const u16* __restrict__ ssm_k, const u16* __restrict__ ssm_D,
                                              u16* __restrict__ out, const u16* __restrict__ pq,
                                              const float* __restrict__ part, const float* __restrict__ zpart,
                                              u16* __restrict__ o) {
  __shared__ __attribute__((aligned(16))) u16 sm[CONV_LDS];
  if (blockIdx.x >= 256) {
    const int sub = threadIdx.x >> 8;
    attn_o(pq, part, zpart, o, (blockIdx.x - 256) * 2 + sub, threadIdx.x & 255, sm + sub * 2688);
    return;
  }
  u16 (*kvT)[32 * 72] = reinterpret_cast<u16 (*)[32 * 72]>(sm);
  u16 (*yT)[S_LEN] = reinterpret_cast<u16 (*)[S_LEN]>(sm + 4 * 32 * 72);
  u16 (*hr)[2][HR] = reinterpret_cast<u16 (*)[2][HR]>(sm + 4 * 32 * 72 + 4 * S_LEN);
  u16* zrow = sm + 4 * 32 * 72 + 4 * S_LEN + 8 * HR;
  const int c0 = ((blockIdx.x & 7) * 32 + (blockIdx.x >> 3)) * 4, tid = threadIdx.x, lane = tid & 63, w = tid >> 6;
  const int ch = w & 3, hw = w >> 2;
  if (tid < 64) zrow[tid] = 0;
  {
    const u16* hsrc = ssm_k + (size_t)(c0 + ch) * S_LEN;
    uint4 u[2]; uint2 nx[2];
#pragma unroll
    for (int i = 0; i < 2; ++i) {
      int idx0 = (lane + 64 * (2 * hw + i)) * 8;
      u[i] = *reinterpret_cast<const uint4*>(hsrc + idx0);
      nx[i] = (idx0 + 8 < S_LEN) ? *reinterpret_cast<const uint2*>(hsrc + idx0 + 8) : make_uint2(0, 0);
    }
    if (hw == 0) {
#pragma unroll
      for (int sft = 0; sft < 2; ++sft) {
        if (lane < 16) hr[ch][sft][lane] = 0;
        hr[ch][sft][2064 + lane] = (sft - 1 - lane >= 0) ? hsrc[sft - 1 - lane] : (u16)0;
        if (lane < HR - 2128) hr[ch][sft][2128 + lane] = 0;
      }
    }
#pragma unroll
    for (int i = 0; i < 2; ++i) {
      int idx0 = (lane + 64 * (2 * hw + i)) * 8;
      unsigned wv[6] = {u[i].x, u[i].y, u[i].z, u[i].w, nx[i].x, nx[i].y};
      unsigned e[12];
#pragma unroll
      for (int t = 0; t < 12; ++t) e[t] = (t & 1) ? (wv[t >> 1] >> 16) : (wv[t >> 1] & 0xffffu);
#pragma unroll
      for (int sft = 0; sft < 2; ++sft) {
        uint4 r = make_uint4(e[sft + 7] | (e[sft + 6] << 16), e[sft + 5] | (e[sft + 4] << 16),
                             e[sft + 3] | (e[sft + 2] << 16), e[sft + 1] | (e[sft] << 16));
        *reinterpret_cast<uint4*>(&hr[ch][sft][2056 - idx0]) = r;
      }
    }
  }
  {
    const int t0 = tid * 4;
    uint2 ku[7], vu[4];
#pragma unroll
    for (int i = 0; i < 7; ++i) {
      int t = t0 - 3 + i;
      ku[i] = make_uint2(0, 0);
      if (t >= 0) ku[i] = *reinterpret_cast<const uint2*>(k + (size_t)t * DM + c0);
    }
#pragma unroll
    for (int r = 0; r < 4; ++r) vu[r] = *reinterpret_cast<const uint2*>(v + (size_t)(t0 + r) * DM + c0);
    auto get = [](uint2 u, int c) {
      unsigned x = (c & 2) ? u.y : u.x;
      return (c & 1) ? __uint_as_float(x & 0xffff0000u) : __uint_as_float(x << 16);
    };
#pragma unroll
    for (int c = 0; c < 4; ++c) {
      float wt[4];
#pragma unroll
      for (int t = 0; t < 4; ++t) wt[t] = bf2f(shift_k[(c0 + c) * 4 + 3 - t]);
      u16 res[4];
#pragma unroll
      for (int r = 0; r < 4; ++r) {
        float ks = 0.f;
#pragma unroll
        for (int t = 0; t < 4; ++t) ks = rbf(ks + rbf(get(ku[r + t], c) * wt[t]));
        res[r] = f2bf(ks * get(vu[r], c));
      }
      *reinterpret_cast<uint2*>(&kvT[c][(t0 >> 6) * 72 + (t0 & 63)]) =
          make_uint2(res[0] | ((unsigned)res[1] << 16), res[2] | ((unsigned)res[3] << 16));
    }
  }
  __syncthreads();
  const int n = lane & 15, g = lane >> 4;
  f32x4 acc[2][2];
#pragma unroll
  for (int a = 0; a < 2; ++a)
#pragma unroll
    for (int b = 0; b < 2; ++b) acc[a][b] = (f32x4){0.f, 0.f, 0.f, 0.f};
  const u16* hbase = &hr[ch][(n + 1) & 1][2064 - ((n + 1) & ~1) + 8 * g - 32 * hw + 32];
  auto ldf = [&](int jj, bf16x8 (&bfr)[4], bf16x8 (&af)[2][2]) {
#pragma unroll
    for (int f = 0; f < 4; ++f) {
      const unsigned* hp = reinterpret_cast<const unsigned*>(hbase - (64 * jj + 16 * f));
      uint4 u = make_uint4(hp[0], hp[1], hp[2], hp[3]);
      bfr[f] = *reinterpret_cast<bf16x8*>(&u);
    }
#pragma unroll
    for (int ab = 0; ab < 2; ++ab) {
      const int a = ab * 16 + n - jj;
#pragma unroll
      for (int kb = 0; kb < 2; ++kb) {
        const u16* ap = (a >= 0) ? &kvT[ch][a * 72 + kb * 32 + g * 8] : &zrow[g * 8];
        af[ab][kb] = *reinterpret_cast<const bf16x8*>(ap);
      }
    }
  };
  auto mm2 = [&](bf16x8 (&bfr)[4], bf16x8 (&af)[2][2]) {
#pragma unroll
    for (int kb = 0; kb < 2; ++kb)
#pragma unroll
      for (int nb = 0; nb < 2; ++nb) {
        acc[0][nb] = __builtin_amdgcn_mfma_f32_16x16x32_bf16(af[0][kb], bfr[nb - 2 * kb + 2], acc[0][nb], 0, 0, 0);
        acc[1][nb] = __builtin_amdgcn_mfma_f32_16x16x32_bf16(af[1][kb], bfr[nb - 2 * kb + 2], acc[1][nb], 0, 0, 0);
      }
  };
  auto mm1 = [&](bf16x8 (&bfr)[4], bf16x8 (&af)[2][2]) {
#pragma unroll
    for (int kb = 0; kb < 2; ++kb)
#pragma unroll
      for (int nb = 0; nb < 2; ++nb)
        acc[1][nb] = __builtin_amdgcn_mfma_f32_16x16x32_bf16(af[1][kb], bfr[nb - 2 * kb + 2], acc[1][nb], 0, 0, 0);
  };
  bf16x8 b0[4], a0[2][2], b1[4], a1[2][2];
  ldf(0, b0, a0);
#pragma unroll 1
  for (int jj = 0; jj < 16; jj += 2) {
    ldf(jj + 1, b1, a1);
    __builtin_amdgcn_sched_barrier(0);
    mm2(b0, a0);
    __builtin_amdgcn_sched_barrier(0);
    ldf(jj + 2, b0, a0);
    __builtin_amdgcn_sched_barrier(0);
    mm2(b1, a1);
    __builtin_amdgcn_sched_barrier(0);
  }
#pragma unroll 1
  for (int jj = 16; jj < 32; jj += 2) {
    ldf(jj + 1, b1, a1);
    __builtin_amdgcn_sched_barrier(0);
    mm1(b0, a0);
    __builtin_amdgcn_sched_barrier(0);
    ldf(jj + 2 < 32 ? jj + 2 : 31, b0, a0);
    __builtin_amdgcn_sched_barrier(0);
    mm1(b1, a1);
    __builtin_amdgcn_sched_barrier(0);
  }
#pragma unroll
  for (int ab = 0; ab < 2; ++ab)
#pragma unroll
    for (int nb = 0; nb < 2; ++nb)
#pragma unroll
      for (int j = 0; j < 4; ++j) yT[ch][(ab * 16 + g * 4 + j) * 64 + (hw * 2 + nb) * 16 + n] = f2bf(acc[ab][nb][j]);
  {
    const int t0 = tid * 4;
    uint2 qu[4];
#pragma unroll
    for (int r = 0; r < 4; ++r) qu[r] = *reinterpret_cast<const uint2*>(q + (size_t)(t0 + r) * DM + c0);
    __syncthreads();
    u16 res[4][4];
#pragma unroll
    for (int c = 0; c < 4; ++c) {
      uint2 a = *reinterpret_cast<const uint2*>(&yT[c][t0]);
      uint2 b = *reinterpret_cast<const uint2*>(&kvT[c][(t0 >> 6) * 72 + (t0 & 63)]);
      const float D = bf2f(ssm_D[c0 + c]);
#pragma unroll
      for (int r = 0; r < 4; ++r) {
        unsigned ya = (r & 2) ? a.y : a.x, kb = (r & 2) ? b.y : b.x, qx = (c & 2) ? qu[r].y : qu[r].x;
        float yf = (r & 1) ? __uint_as_float(ya & 0xffff0000u) : __uint_as_float(ya << 16);
        float kf = (r & 1) ? __uint_as_float(kb & 0xffff0000u) : __uint_as_float(kb << 16);
        float qf = (c & 1) ? __uint_as_float(qx & 0xffff0000u) : __uint_as_float(qx << 16);
        res[r][c] = f2bf(qf * rbf(yf + rbf(kf * D)));
      }
    }
#pragma unroll
    for (int r = 0; r < 4; ++r)
      *reinterpret_cast<uint2*>(out + (size_t)(t0 + r) * (2 * DM) + DM + c0) =
          make_uint2(res[r][0] | ((unsigned)res[r][1] << 16), res[r][2] | ((unsigned)res[r][3] << 16));
  }
}

extern "C" void kernel_launch(void* const* d_in, const int* in_sizes, int n_in, void* d_out, int out_size,
                              void* d_ws, size_t ws_size, hipStream_t stream) {
  const u16* x = (const u16*)d_in[0];
  const u16* wqkv = (const u16*)d_in[1];
  const u16* wo = (const u16*)d_in[2];
  u16* out = (u16*)d_out;
  char* ws = (char*)d_ws;
  u16* pq = (u16*)ws;
  u16* o = (u16*)(ws + (4u << 20));
  float* part = (float*)(ws + (8u << 20));
  float* zpart = (float*)(ws + (10u << 20));
  k_qkv<<<256, 512, 0, stream>>>(x, wqkv, pq, part, zpart);
  k_conv<<<384, 512, 0, stream>>>((const u16*)d_in[3], (const u16*)d_in[4], (const u16*)d_in[5],
                                  (const u16*)d_in[6], (const u16*)d_in[7], (const u16*)d_in[8], out, pq, part,
                                  zpart, o);
  k_out<<<256, 256, 0, stream>>>(o, wo, out);
}
```
